```python
import torch

def _softmax(x, axis=-1):
    return torch.softmax(x, dim=axis)
def _gelu(x):
    return torch.nn.functional.gelu(x, approximate="tanh")
def _silu(x):
    return torch.nn.functional.silu(x)
def _sigmoid(x):
    return torch.sigmoid(x)
def _layernorm(x, axis=-1, eps=1e-5):
    m = x.mean(dim=axis, keepdim=True)
    v = x.var(dim=axis, keepdim=True, unbiased=False)
    return (x - m) / torch.sqrt(v + eps)
def _scatter_add(tgt, idx, src, axis=0):
    return tgt.clone().index_add_(axis, idx, src)
def _segment_sum(src, seg, n):
    out = torch.zeros((n,) + src.shape[1:], dtype=src.dtype)
    return out.index_add_(0, seg, src)
def _pad(x, pw):
    flat = tuple(v for pair in reversed(pw) for v in pair)
    return torch.nn.functional.pad(x, flat)

kernel_name = 'sequential_preLN__gqa_block_cached__mixer_token_mlp__decode'

def setup_inputs():

    _w = (lambda a: (lambda t: t.to(torch.bfloat16) if t.is_floating_point() else t)(torch.as_tensor(a)))
    def _A_setup():
        B, Sq, D = 1, 1, 512
        Hq, Hkv, Sk, Dh = 32, 8, 65536, 128
        assert Hq % Hkv == 0, (Hq, Hkv)
        return {
            "x": _w((lambda *s: torch.randn(*s, dtype=torch.bfloat16) * 0.1)(B, Sq, D)),
            "k_cache": _w((lambda *s: torch.randn(*s, dtype=torch.bfloat16) * 1.6)(B, Hkv, Sk, Dh)),
            "v_cache": _w((lambda *s: torch.randn(*s, dtype=torch.bfloat16) * 1.6)(B, Hkv, Sk, Dh)),
            "wq": _w((lambda *s: torch.randn(*s, dtype=torch.bfloat16) * 1.6)(D, Hq * Dh) * (D ** -0.5)),
            "wo": _w((lambda *s: torch.randn(*s, dtype=torch.bfloat16) * 1.6)(Hq * Dh, D) * ((Hq * Dh) ** -0.5)),
        }
    _A_in = _A_setup()

    def _B_setup():
        B, S, D, F = 1, 1, 512, 1792
        return {
            "x": _w((lambda *s: torch.randn(*s, dtype=torch.bfloat16) * 0.1)(B, S, D)),
            "w1": _w((lambda *s: torch.randn(*s, dtype=torch.bfloat16) * 1.6)(S, F) * (S ** -0.5)),
            "b1": _w((lambda *s: torch.randn(*s, dtype=torch.bfloat16) * 1.6)(F) * 0.02),
            "w2": _w((lambda *s: torch.randn(*s, dtype=torch.bfloat16) * 1.6)(F, S) * (F ** -0.5)),
            "b2": _w((lambda *s: torch.randn(*s, dtype=torch.bfloat16) * 1.6)(S) * 0.02),
        }
    _B_in = _B_setup()

    return {
        "A_x": _A_in["x"],
        "A_k_cache": _A_in["k_cache"],
        "A_v_cache": _A_in["v_cache"],
        "A_wq": _A_in["wq"],
        "A_wo": _A_in["wo"],
        "B_x": _B_in["x"],
        "B_w1": _B_in["w1"],
        "B_b1": _B_in["b1"],
        "B_w2": _B_in["w2"],
        "B_b2": _B_in["b2"],
    }

def reference(A_x, A_k_cache, A_v_cache, A_wq, A_wo, B_x, B_w1, B_b1, B_w2, B_b2):
    def _A_ref(x, k_cache, v_cache, wq, wo):
        B, Sq, D = x.shape
        Hq, Dh = 32, 128
        Hkv = k_cache.shape[1]
        rep = Hq // Hkv
        q = (lambda x, *d: x.permute(*d))((x @ wq).reshape(B, Sq, Hq, Dh), 0, 2, 1, 3)
        k = (lambda x, n, axis: torch.repeat_interleave(x, n, dim=axis))(k_cache, rep, axis=1)
        v = (lambda x, n, axis: torch.repeat_interleave(x, n, dim=axis))(v_cache, rep, axis=1)
        scores = torch.einsum("bhqd,bhkd->bhqk", q, k) * (Dh ** -0.5)
        p = _softmax(scores, axis=-1)
        o = torch.einsum("bhqk,bhkd->bhqd", p, v)
        return (lambda x, *d: x.permute(*d))(o, 0, 2, 1, 3).reshape(B, Sq, Hq * Dh) @ wo

    def _B_ref(x, w1, b1, w2, b2):
        B, S, D = x.shape
        h = _layernorm(x, axis=-1)
        u = torch.einsum("bsd,sf->bdf", h, w1) + b1
        u = _gelu(u)
        y = torch.einsum("bdf,fs->bds", u, w2) + b2
        return (lambda x, *d: x.permute(*d))(y, 0, 2, 1)

    x = A_x
    h = x + _A_ref(_layernorm(x, axis=-1), A_k_cache, A_v_cache, A_wq, A_wo)
    h2 = h + _B_ref(_layernorm(h, axis=-1), B_w1, B_b1, B_w2, B_b2)
    return h2
```

```cpp
#include <hip/hip_runtime.h>
#include <hip/hip_bf16.h>
#include <stdint.h>

#define GAS __attribute__((address_space(1)))
#define LAS __attribute__((address_space(3)))
typedef unsigned u32x4 __attribute__((ext_vector_type(4)));
typedef unsigned u32x2 __attribute__((ext_vector_type(2)));
typedef float f32x4 __attribute__((ext_vector_type(4)));
typedef short bf16x8 __attribute__((ext_vector_type(8)));
typedef unsigned short u16;

#define MIMIC 0

constexpr int D = 512, HQ = 32, HKV = 8, SK = 65536, DH = 128, FF = 1792;
constexpr int NWG = 256, NSPLIT = 32, KEYS_PER_WG = SK / NSPLIT;
constexpr int NWAVE = 8, KEYS_PER_WAVE = KEYS_PER_WG / NWAVE;
constexpr int NTILE = KEYS_PER_WAVE / 32;

__device__ __forceinline__ float bf2f(unsigned b) { return __builtin_bit_cast(float, b << 16); }
__device__ __forceinline__ unsigned pk_bf16(float lo, float hi) {
  unsigned r; asm("v_cvt_pk_bf16_f32 %0, %1, %2" : "=v"(r) : "v"(lo), "v"(hi)); return r;
}
__device__ __forceinline__ float rbf(float x) {
  return x;
}
__device__ __forceinline__ f32x4 mfma16(u32x4 a, u32x4 b, f32x4 c) {
  return __builtin_amdgcn_mfma_f32_16x16x32_bf16(__builtin_bit_cast(bf16x8, a), __builtin_bit_cast(bf16x8, b), c, 0, 0, 0);
}
__device__ __forceinline__ float wave_sum(float v) {
#pragma unroll
  for (int m = 32; m >= 1; m >>= 1) v += __shfl_xor(v, m);
  return v;
}
__device__ __forceinline__ float xg_max(float x) {
  auto s = __builtin_amdgcn_permlane16_swap(__float_as_uint(x), __float_as_uint(x), false, false);
  x = fmaxf(__uint_as_float(s[0]), __uint_as_float(s[1]));
  auto t = __builtin_amdgcn_permlane32_swap(__float_as_uint(x), __float_as_uint(x), false, false);
  return fmaxf(__uint_as_float(t[0]), __uint_as_float(t[1]));
}
__device__ __forceinline__ float xg_sum(float x) {
  auto s = __builtin_amdgcn_permlane16_swap(__float_as_uint(x), __float_as_uint(x), false, false);
  x = __uint_as_float(s[0]) + __uint_as_float(s[1]);
  auto t = __builtin_amdgcn_permlane32_swap(__float_as_uint(x), __float_as_uint(x), false, false);
  return __uint_as_float(t[0]) + __uint_as_float(t[1]);
}

__device__ __forceinline__ void ln8(float (&v)[8]) {
  float s = 0.f;
#pragma unroll
  for (int i = 0; i < 8; ++i) s += v[i];
  s = wave_sum(s);
  const float mean = s * (1.0f / D);
  float q = 0.f;
#pragma unroll
  for (int i = 0; i < 8; ++i) { const float d = v[i] - mean; q += d * d; }
  q = wave_sum(q);
  const float mb = rbf(mean), vb = rbf(q * (1.0f / D));
  const float den = rbf(sqrtf(rbf(vb + 1e-5f)));
#pragma unroll
  for (int i = 0; i < 8; ++i) v[i] = rbf(rbf(v[i] - mb) / den);
}

__global__ void __launch_bounds__(256) k_qproj(const u16* __restrict__ x, const u16* __restrict__ wq, u16* __restrict__ qout) {
  __shared__ float xs[D];
  __shared__ float red[4][16];
  const int tid = threadIdx.x, lane = tid & 63, wave = tid >> 6, b = blockIdx.x;
  u32x4 w[4];
#pragma unroll
  for (int i = 0; i < 4; ++i) w[i] = *(const u32x4*)(wq + (size_t)((tid >> 1) + 128 * i) * (HQ * DH) + 16 * b + 8 * (tid & 1));
  if (wave == 0) {
    const u32x4 xv = *(const u32x4*)(x + 8 * lane);
    float v[8];
#pragma unroll
    for (int i = 0; i < 4; ++i) { v[2 * i] = bf2f(xv[i] & 0xffffu); v[2 * i + 1] = bf2f(xv[i] >> 16); }
    ln8(v);
#pragma unroll
    for (int i = 0; i < 8; ++i) xs[8 * lane + i] = v[i];
  }
  __syncthreads();
  float acc[8];
#pragma unroll
  for (int j = 0; j < 8; ++j) acc[j] = 0.f;
#pragma unroll
  for (int i = 0; i < 4; ++i) {
    const float xr = xs[(tid >> 1) + 128 * i];
#pragma unroll
    for (int j = 0; j < 4; ++j) { acc[2 * j] += xr * bf2f(w[i][j] & 0xffffu); acc[2 * j + 1] += xr * bf2f(w[i][j] >> 16); }
  }
#pragma unroll
  for (int j = 0; j < 8; ++j) {
#pragma unroll
    for (int m = 32; m >= 2; m >>= 1) acc[j] += __shfl_xor(acc[j], m);
  }
  if (lane < 2) {
#pragma unroll
    for (int j = 0; j < 8; ++j) red[wave][8 * lane + j] = acc[j];
  }
  __syncthreads();
  if (tid < 16) {
    const float s = (red[0][tid] + red[1][tid]) + (red[2][tid] + red[3][tid]);
    qout[16 * b + tid] = (u16)(pk_bf16(s, s) & 0xffffu);
  }
}

__device__ __forceinline__ u32x4 ldnt(const u16* p) { return __builtin_nontemporal_load((const u32x4*)p); }

struct Tile { u32x4 k[8]; u32x4 v[8]; };
__device__ __forceinline__ void load_tile(Tile& t, const u16* kp, const u16* vp) {
#pragma unroll
  for (int i = 0; i < 8; ++i) t.k[i] = ldnt(kp + (i >> 2) * 16 * DH + (i & 3) * 32);
#pragma unroll
  for (int i = 0; i < 8; ++i) t.v[i] = ldnt(vp + ((i >> 2) * 16 + (i & 3)) * DH);
}

__global__ void __launch_bounds__(512, 2) k_attn(const u16* __restrict__ K, const u16* __restrict__ V, const u16* __restrict__ q, float* __restrict__ part) {
  __shared__ float sm[NWAVE][4][DH + 4];
  const int tid = threadIdx.x, lane = tid & 63, wave = __builtin_amdgcn_readfirstlane(tid >> 6), b = blockIdx.x;
  const int g = lane >> 4, c = lane & 15;
  const int kvh = b & 7, split = b >> 3;
  const size_t key0 = (size_t)kvh * SK + (size_t)split * KEYS_PER_WG + (size_t)wave * KEYS_PER_WAVE;
  const u16* kp = K + (key0 + c) * DH + 8 * g;
  const u16* vp = V + (key0 + 4 * g) * DH + 8 * c;
  Tile t[2];
  u32x4 qf[4];
#pragma unroll
  for (int s = 0; s < 4; ++s) {
    u32x4 z = {0u, 0u, 0u, 0u};
    if (c < 4) z = *(const u32x4*)(q + (kvh * 4 + c) * DH + 32 * s + 8 * g);
    qf[s] = z;
  }
  load_tile(t[0], kp, vp);
  load_tile(t[1], kp + 32 * DH, vp + 32 * DH);
  __builtin_amdgcn_sched_barrier(0);
  f32x4 o[8];
#pragma unroll
  for (int d = 0; d < 8; ++d) o[d] = f32x4{0.f, 0.f, 0.f, 0.f};
  float m = -INFINITY, l = 0.f;
  constexpr float LOG2E = 1.4426950408889634f;
  constexpr float SCALE = 0.08838834764831845f;
#pragma unroll
  for (int it = 0; it < NTILE; ++it) {
    Tile& cur = t[it & 1];
    f32x4 s0 = {0.f, 0.f, 0.f, 0.f}, s1 = {0.f, 0.f, 0.f, 0.f};
#pragma unroll
    for (int s = 0; s < 4; ++s) { s0 = mfma16(cur.k[s], qf[s], s0); s1 = mfma16(cur.k[4 + s], qf[s], s1); }
    float sc[8];
#pragma unroll
    for (int r = 0; r < 4; ++r) { sc[r] = rbf(rbf(s0[r]) * SCALE); sc[4 + r] = rbf(rbf(s1[r]) * SCALE); }
    float mx = fmaxf(fmaxf(fmaxf(sc[0], sc[1]), fmaxf(sc[2], sc[3])), fmaxf(fmaxf(sc[4], sc[5]), fmaxf(sc[6], sc[7])));
    mx = xg_max(mx);
    const float mn = fmaxf(m, mx);
    const float alpha = __builtin_amdgcn_exp2f((m - mn) * LOG2E);
    m = mn;
    float p[8], ps = 0.f;
#pragma unroll
    for (int r = 0; r < 8; ++r) { p[r] = __builtin_amdgcn_exp2f((sc[r] - mn) * LOG2E); ps += p[r]; }
    l = l * alpha + ps;
    u32x4 pf = {pk_bf16(p[0], p[1]), pk_bf16(p[2], p[3]), pk_bf16(p[4], p[5]), pk_bf16(p[6], p[7])};
#pragma unroll
    for (int d = 0; d < 8; ++d) {
      u32x4 a;
#pragma unroll
      for (int jj = 0; jj < 4; ++jj) a[jj] = __builtin_amdgcn_perm(cur.v[2 * jj + 1][d >> 1], cur.v[2 * jj][d >> 1], (d & 1) ? 0x07060302u : 0x05040100u);
      o[d] = mfma16(a, pf, o[d] * alpha);
    }
    if (it + 2 < NTILE) load_tile(cur, kp + (it + 2) * 32 * DH, vp + (it + 2) * 32 * DH);
    __builtin_amdgcn_sched_barrier(0);
  }
  l = xg_sum(l);
  if (c < 4) {
#pragma unroll
    for (int r = 0; r < 4; ++r) {
      float* dst = &sm[wave][c][8 * (4 * g + r)];
      *(f32x4*)dst = f32x4{o[0][r], o[1][r], o[2][r], o[3][r]};
      *(f32x4*)(dst + 4) = f32x4{o[4][r], o[5][r], o[6][r], o[7][r]};
    }
    if (g == 0) { sm[wave][c][DH] = m; sm[wave][c][DH + 1] = l; }
  }
  __syncthreads();
  {
    const int h = tid >> 7, dim = tid & 127;
    float M = -INFINITY;
#pragma unroll
    for (int w = 0; w < NWAVE; ++w) M = fmaxf(M, sm[w][h][DH]);
    float L = 0.f, acc = 0.f;
#pragma unroll
    for (int w = 0; w < NWAVE; ++w) {
      const float e = __builtin_amdgcn_exp2f((sm[w][h][DH] - M) * LOG2E);
      L += e * sm[w][h][DH + 1]; acc += e * sm[w][h][dim];
    }
    float* dst = part + ((size_t)(kvh * 4 + h) * NSPLIT + split) * 132;
    dst[dim] = acc;
    if (dim == 0) { dst[128] = M; dst[129] = L; }
  }
}

__global__ void __launch_bounds__(256) k_comb(const float* __restrict__ part, const u16* __restrict__ wo, float* __restrict__ ypart) {
  __shared__ float os[DH];
  __shared__ float tmp[2][DH];
  __shared__ float red[4][64];
  const int tid = threadIdx.x, lane = tid & 63, wave = tid >> 6, b = blockIdx.x, h = b >> 3, cg = b & 7;
  constexpr float LOG2E = 1.4426950408889634f;
  u32x4 w[4];
#pragma unroll
  for (int i = 0; i < 4; ++i) w[i] = *(const u32x4*)(wo + (size_t)(h * DH + (tid >> 3) + 32 * i) * D + cg * 64 + 8 * (tid & 7));
  const float* ph = part + (size_t)h * NSPLIT * 132;
  float M = -INFINITY;
  for (int s = 0; s < NSPLIT; ++s) M = fmaxf(M, ph[s * 132 + 128]);
  float L = 0.f, acc = 0.f;
  const int dim = tid & 127, half = tid >> 7;
  for (int s = 0; s < NSPLIT; ++s) {
    const float e = __builtin_amdgcn_exp2f((ph[s * 132 + 128] - M) * LOG2E);
    L += e * ph[s * 132 + 129];
    if ((s & 1) == half) acc += e * ph[s * 132 + dim];
  }
  tmp[half][dim] = acc;
  __syncthreads();
  if (tid < DH) os[tid] = rbf((tmp[0][tid] + tmp[1][tid]) / L);
  __syncthreads();
  float a[8];
#pragma unroll
  for (int j = 0; j < 8; ++j) a[j] = 0.f;
#pragma unroll
  for (int i = 0; i < 4; ++i) {
    const float ov = os[(tid >> 3) + 32 * i];
#pragma unroll
    for (int j = 0; j < 4; ++j) { a[2 * j] += ov * bf2f(w[i][j] & 0xffffu); a[2 * j + 1] += ov * bf2f(w[i][j] >> 16); }
  }
#pragma unroll
  for (int j = 0; j < 8; ++j) {
#pragma unroll
    for (int m = 32; m >= 8; m >>= 1) a[j] += __shfl_xor(a[j], m);
  }
  if (lane < 8) {
#pragma unroll
    for (int j = 0; j < 8; ++j) red[wave][8 * lane + j] = a[j];
  }
  __syncthreads();
  if (tid < 64) ypart[h * D + cg * 64 + tid] = (red[0][tid] + red[1][tid]) + (red[2][tid] + red[3][tid]);
}

__device__ __forceinline__ float gelu_tanh(float u) {
  const float kBeta = 0.7978845608028654f, kKappa = 0.044715f;
  const float inner = kBeta * (u + kKappa * u * u * u);
  const float e = __builtin_amdgcn_exp2f(inner * (2.0f * 1.4426950408889634f));
  const float th = 1.0f - 2.0f / (e + 1.0f);
  return 0.5f * u * (1.0f + th);
}
__global__ void __launch_bounds__(256) k_final(const float* __restrict__ ypart, const u16* __restrict__ x, const u16* __restrict__ w1, const u16* __restrict__ b1,
                                               const u16* __restrict__ w2, const u16* __restrict__ b2, u16* __restrict__ out) {
  __shared__ float hs[D], hn[D];
  __shared__ float red[4][2];
  const int tid = threadIdx.x, lane = tid & 63, wave = tid >> 6, b = blockIdx.x;
#pragma unroll
  for (int k = 0; k < 2; ++k) {
    const int d = tid + 256 * k;
    float s = 0.f;
    for (int h = 0; h < HQ; ++h) s += ypart[h * D + d];
    hs[d] = rbf(bf2f(x[d]) + rbf(s));
  }
  __syncthreads();
  if (wave == 0) {
    float v[8];
#pragma unroll
    for (int i = 0; i < 8; ++i) v[i] = hs[8 * lane + i];
    ln8(v); ln8(v);
#pragma unroll
    for (int i = 0; i < 8; ++i) hn[8 * lane + i] = v[i];
  }
  __syncthreads();
  const float t0 = hn[2 * b], t1 = hn[2 * b + 1];
  float a0 = 0.f, a1 = 0.f;
#pragma unroll
  for (int i = 0; i < FF / 256; ++i) {
    const int f = tid + 256 * i;
    const float ww1 = bf2f(w1[f]), bb1 = bf2f(b1[f]), ww2 = bf2f(w2[f]);
    a0 += rbf(gelu_tanh(rbf(rbf(t0 * ww1) + bb1))) * ww2;
    a1 += rbf(gelu_tanh(rbf(rbf(t1 * ww1) + bb1))) * ww2;
  }
  a0 = wave_sum(a0); a1 = wave_sum(a1);
  if (lane == 0) { red[wave][0] = a0; red[wave][1] = a1; }
  __syncthreads();
  if (tid < 2) {
    const float y = (red[0][tid] + red[1][tid]) + (red[2][tid] + red[3][tid]);
    const float yy = rbf(rbf(y) + bf2f(b2[0]));
    const float r = hs[2 * b + tid] + yy;
    out[2 * b + tid] = (u16)(pk_bf16(r, r) & 0xffffu);
  }
}

extern "C" void kernel_launch(void* const* d_in, const int* in_sizes, int n_in, void* d_out, int out_size, void* d_ws, size_t ws_size, hipStream_t stream) {
  const u16* x = (const u16*)d_in[0];
  const u16* K = (const u16*)d_in[1];
  const u16* V = (const u16*)d_in[2];
  const u16* wq = (const u16*)d_in[3];
  const u16* wo = (const u16*)d_in[4];
  const u16* w1 = (const u16*)d_in[6];
  const u16* b1 = (const u16*)d_in[7];
  const u16* w2 = (const u16*)d_in[8];
  const u16* b2 = (const u16*)d_in[9];
  unsigned char* ws = (unsigned char*)d_ws;
  u16* q = (u16*)ws;
  float* part = (float*)(ws + 65536);
  float* ypart = (float*)(ws + (1 << 20));
  k_qproj<<<256, 256, 0, stream>>>(x, wq, q);
  k_attn<<<NWG, 512, 0, stream>>>(K, V, q, part);
  k_comb<<<256, 256, 0, stream>>>(part, wo, ypart);
  k_final<<<256, 256, 0, stream>>>(ypart, x, w1, b1, w2, b2, (u16*)d_out);
}
```

```cpp
#include <hip/hip_runtime.h>
#include <hip/hip_bf16.h>
#include <stdint.h>

#define GAS __attribute__((address_space(1)))
#define LAS __attribute__((address_space(3)))
typedef unsigned u32x4 __attribute__((ext_vector_type(4)));
typedef unsigned u32x2 __attribute__((ext_vector_type(2)));
typedef float f32x4 __attribute__((ext_vector_type(4)));
typedef float f32x2 __attribute__((ext_vector_type(2)));
typedef short bf16x8 __attribute__((ext_vector_type(8)));
typedef short s16x4 __attribute__((ext_vector_type(4)));
typedef __bf16 bf16x2 __attribute__((ext_vector_type(2)));
typedef unsigned short u16;
typedef unsigned long long u64;

#define STAMPS 0
#define INFL 24
#define QD 100
#define RETIRE 8
#define PSLEEP 1
#define QSLEEP 4
#define CD 50
#define BD 0
#define XMAP 0
#define NSTAT 60

constexpr int D = 512, HQ = 32, SK = 65536, DH = 128, FF = 1792;
constexpr int NWG = 256, NSPLIT = 32;
constexpr int NT = 64;
constexpr int NSLOT = 8, SLOT = 16384;
constexpr int NC = 6;
constexpr int HELPER = 5;
constexpr int L_RING = 0;
constexpr int L_WO = NSLOT * SLOT;
constexpr int L_CTRL = L_WO + 16384;
constexpr int C_KL = 0, C_VL = 1, C_NTOT = 2, C_TICKET = 4, C_QCNT = 5, C_QREADY = 6, C_FREE = 16;
constexpr int L_QS = L_CTRL + 256;
constexpr int L_QP = L_QS + 1024;
constexpr int L_MRG = L_QP + 1024;
constexpr int L_MML = L_MRG + NC * 4 * 528;
constexpr int NPOOL = NSPLIT * (NT - NSTAT);
constexpr int L_OB = L_MML + 256;
constexpr int L_STAT = L_OB + 256;
constexpr int L_RED = L_STAT + 64;
constexpr int L_ST = L_RED + 64;
constexpr int L_END = L_ST + (STAMPS ? 512 : 0);
static_assert(L_END <= 160 * 1024, "LDS");
constexpr int PREC = 640;
constexpr int W_EPOCH = 0, W_POOL = 4096, W_Y = 65536, W_QG = 131072, W_PA = 262144, W_END = W_PA + HQ * NSPLIT * PREC;
constexpr int W_STAMP = 1 << 20;
constexpr unsigned SENT = 0xFFFFFFFFu;
constexpr int AUX_SC1 = 16;
constexpr float LOG2E = 1.4426950408889634f;
constexpr int SPIN_CAP = 1 << 18;

__device__ __forceinline__ float bf2f(unsigned b) { return __builtin_bit_cast(float, b << 16); }
__device__ __forceinline__ float bfhi(unsigned b) { return __builtin_bit_cast(float, b & 0xffff0000u); }
__device__ __forceinline__ f32x2 unpk(unsigned u) { return f32x2{bf2f(u), bfhi(u)}; }
__device__ __forceinline__ unsigned pk_bf16(float lo, float hi) {
  f32x2 v = {lo, hi};
  return __builtin_bit_cast(unsigned, __builtin_convertvector(v, bf16x2));
}
__device__ __forceinline__ float rbf(float x) { return bf2f(pk_bf16(x, x)); }
__device__ __forceinline__ f32x2 rbf2(f32x2 v) { return unpk(pk_bf16(v[0], v[1])); }
__device__ __forceinline__ f32x4 mfma16(u32x4 a, u32x4 b, f32x4 c) {
  return __builtin_amdgcn_mfma_f32_16x16x32_bf16(__builtin_bit_cast(bf16x8, a), __builtin_bit_cast(bf16x8, b), c, 0, 0, 0);
}
template <int CTRL> __device__ __forceinline__ float dpp(float x) {
  return __builtin_bit_cast(float, __builtin_amdgcn_mov_dpp(__builtin_bit_cast(int, x), CTRL, 0xf, 0xf, true));
}
constexpr int XOR1 = 0xB1, XOR2 = 0x4E, XOR7 = 0x141, XOR8 = 0x128;
__device__ __forceinline__ float swap16_add(float a, float b) {
  auto s = __builtin_amdgcn_permlane16_swap(__float_as_uint(a), __float_as_uint(b), false, false);
  return __uint_as_float(s[0]) + __uint_as_float(s[1]);
}
__device__ __forceinline__ float swap32_add(float a, float b) {
  auto s = __builtin_amdgcn_permlane32_swap(__float_as_uint(a), __float_as_uint(b), false, false);
  return __uint_as_float(s[0]) + __uint_as_float(s[1]);
}
__device__ __forceinline__ float xg_max(float x) {
  auto s = __builtin_amdgcn_permlane16_swap(__float_as_uint(x), __float_as_uint(x), false, false);
  x = fmaxf(__uint_as_float(s[0]), __uint_as_float(s[1]));
  auto t = __builtin_amdgcn_permlane32_swap(__float_as_uint(x), __float_as_uint(x), false, false);
  return fmaxf(__uint_as_float(t[0]), __uint_as_float(t[1]));
}
__device__ __forceinline__ float vmax(float a, float b) { float r; asm("v_max_f32 %0, %1, %2" : "=v"(r) : "v"(a), "v"(b)); return r; }
__device__ __forceinline__ float vmax3(float a, float b, float c) { float r; asm("v_max3_f32 %0, %1, %2, %3" : "=v"(r) : "v"(a), "v"(b), "v"(c)); return r; }
__device__ __forceinline__ float xg_max2(float x) {
  float t;
  asm("v_mov_b32 %1, %0\n\ts_nop 1\n\tv_permlane16_swap_b32 %0, %1\n\tv_max_f32 %0, %0, %1\n\tv_mov_b32 %1, %0\n\ts_nop 1\n\tv_permlane32_swap_b32 %0, %1\n\tv_max_f32 %0, %0, %1"
      : "+v"(x), "=&v"(t));
  return x;
}
__device__ __forceinline__ float xor8_max(float x) {
  asm("s_nop 1\n\tv_max_f32_dpp %0, %0, %0 row_ror:8 row_mask:0xf bank_mask:0xf" : "+v"(x));
  return x;
}
__device__ __forceinline__ float xg_sum(float x) { x = swap16_add(x, x); return swap32_add(x, x); }
__device__ __forceinline__ float rdlane(float v, int l) { return __builtin_bit_cast(float, __builtin_amdgcn_readlane(__builtin_bit_cast(int, v), l)); }
__device__ __forceinline__ float rdfirst(float v) { return __builtin_bit_cast(float, __builtin_amdgcn_readfirstlane(__builtin_bit_cast(int, v))); }
__device__ __forceinline__ void wave_sum2(float a, float b, float& sa, float& sb) {
  float v = swap32_add(a, b);
  v += dpp<XOR1>(v); v += dpp<XOR2>(v); v += dpp<XOR7>(v); v += dpp<XOR8>(v);
  v = swap16_add(v, v);
  sa = rdlane(v, 0); sb = rdlane(v, 32);
}
__device__ __forceinline__ float ln_rden(float var) { return __builtin_amdgcn_rcpf(rbf(__builtin_amdgcn_sqrtf(rbf(rbf(var) + 1e-5f)))); }
__device__ __forceinline__ f32x2 ln_apply2(f32x2 v, float mb, float rden) { return rbf2(rbf2(v - mb) * rden); }
__device__ __forceinline__ f32x2 gelu2(f32x2 u) {
  constexpr float C0 = 0.7978845608028654f * 2.0f * LOG2E, C1 = C0 * 0.044715f;
  const f32x2 z = u * (u * u * C1 + C0);
  const f32x2 e = {__builtin_amdgcn_exp2f(z[0]), __builtin_amdgcn_exp2f(z[1])};
  const f32x2 e1 = e + 1.0f;
  const f32x2 r = {__builtin_amdgcn_rcpf(e1[0]), __builtin_amdgcn_rcpf(e1[1])};
  return u - u * r;
}
__device__ __forceinline__ unsigned umax4(u32x4 v) { return max(max(v[0], v[1]), max(v[2], v[3])); }
__device__ __forceinline__ u64 rt() { return __builtin_amdgcn_s_memrealtime(); }
#define LDS_WAIT() asm volatile("s_waitcnt lgkmcnt(0)" ::: "memory")
#define CFENCE() asm volatile("" ::: "memory")
#define BAR() do { __builtin_amdgcn_sched_barrier(0); LDS_WAIT(); __builtin_amdgcn_s_barrier(); CFENCE(); __builtin_amdgcn_sched_barrier(0); } while (0)
#define LSPIN(cond) do { int sp_ = 0; _Pragma("nounroll") while (cond) { __builtin_amdgcn_s_sleep(1); if (++sp_ > SPIN_CAP) break; } __builtin_amdgcn_fence(__ATOMIC_ACQUIRE, "workgroup"); } while (0)
#define STAMP(i) do {} while (0)
#define STAMPC(i) do {} while (0)
#define STAMP_OUT() do {} while (0)
#define STR2(x) #x
#define STR(x) STR2(x)

__global__ void __launch_bounds__(512, 2)
k_fused(const u16* __restrict__ x, const u16* __restrict__ K, const u16* __restrict__ V, const u16* __restrict__ wq, const u16* __restrict__ wo,
        const u16* __restrict__ w1, const u16* __restrict__ b1, const u16* __restrict__ w2, const u16* __restrict__ b2, u16* __restrict__ out,
        unsigned char* __restrict__ ws) {
  extern __shared__ __attribute__((aligned(16))) unsigned char lds[];
  LAS unsigned char* L8 = (LAS unsigned char*)lds;
  volatile LAS unsigned* CTRL = (volatile LAS unsigned*)(L8 + L_CTRL);
  const int tid = threadIdx.x, lane = tid & 63, wave = __builtin_amdgcn_readfirstlane(tid >> 6), b = blockIdx.x;
  const int g = lane >> 4, c = lane & 15;
  const int kvh = XMAP ? (b & 7) : (b >> 5), split = XMAP ? (b >> 3) : (b & 31);
  const int bhead = 4 * (b & 7) + (b >> 6), bcg = (b >> 3) & 7;
  const __amdgpu_buffer_rsrc_t rs = __builtin_amdgcn_make_buffer_rsrc((void*)ws, 0, W_END, 0x00020000);
  {
    const u64 kb = (u64)K + (u64)kvh * (SK * DH * 2) - 3072, vb = (u64)V + (u64)kvh * (SK * DH * 2), pool = (u64)ws + W_POOL + kvh * 256;
    const unsigned vk0 = (4 * (c & 3) + (c >> 2)) * 256 + g * 16 + 3072;
    const unsigned vinit = lane == C_NTOT ? 0x7fffffffu : 0u;
    unsigned sq, st, st1, st2, spk, spv, sa, sb, sm0;
    unsigned vt, va, vc;
#define GL(v, off) "global_load_lds_dwordx4 %[" v "], s[90:91] offset:" off " nt\n\t"
#define PUB(reg, off) "v_mov_b32 %[vt], %[" reg "]\n\tds_write_b32 %[vc], %[vt] offset:" off "\n\t"
#define LST(i)
#define LSTQ(qq, i)
    asm volatile(
        "s_cmp_lg_u32 %[wave], 0\n\t"
        "s_cbranch_scc1 Lld_end\n\t"
        "s_mov_b32 %[sq], 0\n\t"
        "s_mov_b32 %[spk], 0\n\t"
        "s_mov_b32 %[spv], 0\n\t"
        "v_mov_b32 %[vc], %[ctrl]\n\t"
        "v_lshrrev_b32 %[vt], 2, %[vv]\n\t"
        "v_add_u32 %[vt], %[vc], %[vt]\n\t"
        "ds_write_b32 %[vt], %[vinit]\n\t"
        LST("12")
        "Lld_loop:\n\t"
        "s_cmp_lt_u32 %[sq], %[nstat]-2\n\t"
        "s_cbranch_scc1 Lld_noclaim\n\t"
        "s_add_u32 %[sa], %[sq], 2\n\t"
        "s_lshl_b64 exec, 1, %[sa]\n\t"
        "v_mov_b32 %[vt], 0\n\t"
        "v_mov_b32 %[va], 1\n\t"
        "global_atomic_add %[va], %[vt], %[va], %[pool] sc0\n\t"
        "s_mov_b64 exec, -1\n\t"
        "Lld_noclaim:\n\t"
        "s_add_u32 %[st], %[tile0], %[sq]\n\t"
        "s_cmp_lt_u32 %[sq], %[nstat]\n\t"
        "s_cbranch_scc1 Lld_havetile\n\t"
        "v_readlane_b32 %[sa], %[va], %[sq]\n\t"
        "s_nop 0\n\t"
        "s_cmp_ge_u32 %[sa], %[npool]\n\t"
        "s_cbranch_scc1 Lld_tail\n\t"
        "s_and_b32 %[st], %[sa], 31\n\t"
        "s_lshl_b32 %[st], %[st], 6\n\t"
        "s_lshr_b32 %[sa], %[sa], 5\n\t"
        "s_add_u32 %[st], %[st], %[sa]\n\t"
        "s_add_u32 %[st], %[st], %[nstat]\n\t"
        "Lld_havetile:\n\t"
        "s_cmp_lt_u32 %[sq], 8\n\t"
        "s_cbranch_scc1 Lld_free\n\t"
        "s_and_b32 %[sa], %[sq], 7\n\t"
        "s_lshl_b32 %[sa], %[sa], 2\n\t"
        "s_mov_b32 %[sb], %[cap]\n\t"
        "Lld_spin:\n\t"
        "v_add_u32 %[vt], %[sa], %[vc]\n\t"
        "ds_read_b32 %[vt], %[vt] offset:64\n\t"
        "s_waitcnt lgkmcnt(0)\n\t"
        "v_readfirstlane_b32 %[sm0], %[vt]\n\t"
        "s_nop 0\n\t"
        "s_add_u32 %[sm0], %[sm0], 7\n\t"
        "s_cmp_ge_u32 %[sm0], %[sq]\n\t"
        "s_cbranch_scc1 Lld_free\n\t"
        "s_waitcnt vmcnt(0)\n\t"
        "s_mov_b32 %[spk], %[sq]\n\t"
        "s_sub_u32 %[spv], %[sq], 2\n\t"
        PUB("spk", "0") PUB("spv", "4")
        "s_sleep 1\n\t"
        "s_sub_u32 %[sb], %[sb], 1\n\t"
        "s_cmp_lg_u32 %[sb], 0\n\t"
        "s_cbranch_scc1 Lld_spin\n\t"
        "Lld_free:\n\t"
        "s_lshl_b32 %[sa], %[st], 13\n\t"
        "s_add_u32 s90, %[kbl], %[sa]\n\t"
        "s_addc_u32 s91, %[kbh], 0\n\t"
        "s_and_b32 %[sa], %[sq], 7\n\t"
        "s_lshl_b32 m0, %[sa], 14\n\t"
        "s_nop 0\n\t"
        GL("vk0", "0") GL("vk1", "1024") GL("vk2", "2048") GL("vk3", "3072")
        "s_addk_i32 m0, 0x1000\n\t"
        "s_add_u32 s90, s90, 0x1000\n\t"
        "s_addc_u32 s91, s91, 0\n\t"
        GL("vk0", "0") GL("vk1", "1024") GL("vk2", "2048") GL("vk3", "3072")
        "s_cmp_lt_u32 %[sq], 2\n\t"
        "s_cbranch_scc1 Lld_next\n\t"
        "s_waitcnt vmcnt(24)\n\t"
        "s_sub_i32 %[sa], %[sq], 3\n\t"
        "s_max_i32 %[spv], %[spv], %[sa]\n\t"
        PUB("spv", "4")
        "s_lshl_b32 %[sa], %[st2], 13\n\t"
        "s_add_u32 s90, %[vbl], %[sa]\n\t"
        "s_addc_u32 s91, %[vbh], 0\n\t"
        "s_sub_u32 %[sa], %[sq], 2\n\t"
        "s_and_b32 %[sa], %[sa], 7\n\t"
        "s_lshl_b32 %[sa], %[sa], 14\n\t"
        "s_add_u32 m0, %[sa], 0x2000\n\t"
        "s_nop 0\n\t"
        GL("vv", "0") GL("vv", "1024") GL("vv", "2048") GL("vv", "3072")
        "s_addk_i32 m0, 0x1000\n\t"
        "s_add_u32 s90, s90, 0x1000\n\t"
        "s_addc_u32 s91, s91, 0\n\t"
        GL("vv", "0") GL("vv", "1024") GL("vv", "2048") GL("vv", "3072")
        "s_cmp_lg_u32 %[sq], 2\n\t"
        "s_cbranch_scc1 Lld_nobar\n\t"
        LST("13")
        "s_waitcnt lgkmcnt(0)\n\t"
        "s_barrier\n\t"
        "s_branch Lld_next\n\t"
        "Lld_nobar:\n\t"
        "s_waitcnt vmcnt(24)\n\t"
        "s_max_i32 %[spk], %[spk], %[sq]\n\t"
        PUB("spk", "0")
        LSTQ("4", "2") LSTQ("8", "3") LSTQ("16", "4") LSTQ("32", "10") LSTQ("48", "11")
        "Lld_next:\n\t"
        "s_mov_b32 %[st2], %[st1]\n\t"
        "s_mov_b32 %[st1], %[st]\n\t"
        "s_add_u32 %[sq], %[sq], 1\n\t"
        "s_branch Lld_loop\n\t"
        "Lld_tail:\n\t"
        PUB("sq", "8")
        "s_mov_b32 %[sb], 2\n\t"
        "Lld_tail2:\n\t"
        "s_lshl_b32 %[sa], %[st2], 13\n\t"
        "s_add_u32 s90, %[vbl], %[sa]\n\t"
        "s_addc_u32 s91, %[vbh], 0\n\t"
        "s_sub_u32 %[sa], %[sq], %[sb]\n\t"
        "s_and_b32 %[sa], %[sa], 7\n\t"
        "s_lshl_b32 %[sa], %[sa], 14\n\t"
        "s_add_u32 m0, %[sa], 0x2000\n\t"
        "s_nop 0\n\t"
        GL("vv", "0") GL("vv", "1024") GL("vv", "2048") GL("vv", "3072")
        "s_addk_i32 m0, 0x1000\n\t"
        "s_add_u32 s90, s90, 0x1000\n\t"
        "s_addc_u32 s91, s91, 0\n\t"
        GL("vv", "0") GL("vv", "1024") GL("vv", "2048") GL("vv", "3072")
        "s_mov_b32 %[st2], %[st1]\n\t"
        "s_sub_u32 %[sb], %[sb], 1\n\t"
        "s_cmp_lg_u32 %[sb], 0\n\t"
        "s_cbranch_scc1 Lld_tail2\n\t"
        "s_waitcnt vmcnt(32)\n\t"
        "s_sub_i32 %[sa], %[sq], 3\n\t"
        "s_max_i32 %[spv], %[spv], %[sa]\n\t"
        PUB("spv", "4")
        "s_waitcnt vmcnt(24)\n\t"
        PUB("sq", "0")
        LST("5")
        "s_sub_u32 %[spv], %[sq], 2\n\t"
        "s_waitcnt vmcnt(16)\n\t"
        PUB("spv", "4")
        "s_add_u32 %[spv], %[spv], 1\n\t"
        "s_waitcnt vmcnt(8)\n\t"
        PUB("spv", "4")
        "s_waitcnt vmcnt(0)\n\t"
        PUB("sq", "4")
        LST("1")
        "s_waitcnt lgkmcnt(0)\n\t"
        "s_endpgm\n\t"
        "Lld_end:"
        : [sq] "=&s"(sq), [st] "=&s"(st), [st1] "=&s"(st1), [st2] "=&s"(st2), [spk] "=&s"(spk), [spv] "=&s"(spv), [sa] "=&s"(sa), [sb] "=&s"(sb), [sm0] "=&s"(sm0),
          [vt] "=&v"(vt), [va] "=&v"(va), [vc] "=&v"(vc)
        : [wave] "s"(wave), [kbl] "s"((unsigned)kb), [kbh] "s"((unsigned)(kb >> 32)), [vbl] "s"((unsigned)vb), [vbh] "s"((unsigned)(vb >> 32)), [pool] "s"(pool), [tile0] "s"(split * NT), [vk0] "v"(vk0), [vk1] "v"(vk0 + 64 - 1024), [vk2] "v"(vk0 + 128 - 2048),
          [vk3] "v"(vk0 + 192 - 3072), [vv] "v"(lane * 16), [vinit] "v"(vinit), [ctrl] "i"(L_CTRL), [nstat] "i"(NSTAT), [npool] "i"(NPOOL), [cap] "i"(SPIN_CAP), [st0] "i"(L_ST - L_CTRL),
          "s"(x), "s"(wq), "s"(wo), "s"(w1), "s"(b1), "s"(w2), "s"(b2), "s"(out)
        : "memory", "scc", "s90", "s91", "s92", "s93");
  }
  if (wave <= 4) {
    const u32x4 xv = *(const GAS u32x4*)(x + 8 * lane);
    const int row0 = (split & 3) * 128 + 32 * (wave - 1) + 4 * (lane >> 3);
    const u32x2 xo = *(const GAS u32x2*)(x + row0);
    u32x4 wqv[4];
    const u16* wp = wq + (size_t)row0 * (HQ * DH) + kvh * 512 + (split >> 2) * 64 + 8 * (lane & 7);
#pragma unroll
    for (int i = 0; i < 4; ++i) wqv[i] = __builtin_nontemporal_load((const GAS u32x4*)(wp + i * (HQ * DH)));
    BAR();
    const float piv = rdfirst(bf2f(xv[0]));
    const f32x2 d0 = unpk(xv[0]) - piv, d1 = unpk(xv[1]) - piv, d2 = unpk(xv[2]) - piv, d3 = unpk(xv[3]) - piv;
    const f32x2 s = (d0 + d1) + (d2 + d3), qq = (d0 * d0 + d1 * d1) + (d2 * d2 + d3 * d3);
    float S, Q;
    wave_sum2(s[0] + s[1], qq[0] + qq[1], S, Q);
    const float md = S * (1.0f / D);
    const float mb = rbf(piv + md), rden = ln_rden(fmaxf(fmaf(-md, md, Q * (1.0f / D)), 0.f));
    const f32x2 xa = ln_apply2(unpk(xo[0]), mb, rden), xb = ln_apply2(unpk(xo[1]), mb, rden);
    STAMP(10);
    f32x2 a2[4];
#pragma unroll
    for (int j = 0; j < 4; ++j) a2[j] = (unpk(wqv[0][j]) * xa[0] + unpk(wqv[1][j]) * xa[1]) + (unpk(wqv[2][j]) * xb[0] + unpk(wqv[3][j]) * xb[1]);
    float a[8];
#pragma unroll
    for (int j = 0; j < 8; ++j) a[j] = a2[j >> 1][j & 1];
#pragma unroll
    for (int j = 0; j < 4; ++j) a[j] = swap32_add(a[j], a[j + 4]);
#pragma unroll
    for (int j = 0; j < 2; ++j) a[j] = swap16_add(a[j], a[j + 2]);
    { const float keep = (lane & 8) ? a[1] : a[0], send = (lane & 8) ? a[0] : a[1]; a[0] = keep + dpp<XOR8>(send); }
    ((LAS float*)(L8 + L_QP))[(wave - 1) * 64 + 8 * (lane & 7) + (lane >> 3)] = a[0];
    LDS_WAIT();
    if (lane == 0) __hip_atomic_fetch_add((LAS unsigned*)(L8 + L_CTRL) + C_QCNT, 1u, __ATOMIC_RELAXED, __HIP_MEMORY_SCOPE_WORKGROUP);
    STAMP(1);
  } else if (wave == HELPER) {
    const u64 epoch = __hip_atomic_load((GAS u64*)(ws + W_EPOCH), __ATOMIC_RELAXED, __HIP_MEMORY_SCOPE_AGENT);
    const u32x4 sv = {SENT, SENT, SENT, SENT};
    if (lane < 40) {
#pragma unroll
      for (int h = 0; h < 4; ++h) __builtin_amdgcn_raw_buffer_store_b128(sv, rs, W_PA + (((4 * kvh + h) * 5 + (lane >> 3)) * NSPLIT + split) * 128 + (lane & 7) * 16, 0, AUX_SC1);
    }
    if (lane < 16) __builtin_amdgcn_raw_buffer_store_b128(sv, rs, W_Y + (bhead * D + bcg * 64) * 4 + lane * 16, 0, AUX_SC1);
    if (split == 0 && lane == 0) __builtin_amdgcn_raw_buffer_store_b32(0u, rs, W_POOL + kvh * 256, 0, AUX_SC1);
    BAR();
    asm volatile("s_waitcnt vmcnt(0)" ::: "memory");
    STAMP(10);
    const u64 tag = (epoch + 1) << 24;
    const unsigned tlo = (unsigned)tag, thi = (unsigned)(tag >> 32);
    LSPIN(CTRL[C_QCNT] < 4u);
    const LAS float* qp = (const LAS float*)(L8 + L_QP);
    const unsigned qs = __float_as_uint((qp[lane] + qp[64 + lane]) + (qp[128 + lane] + qp[192 + lane]));
    __hip_atomic_store((GAS u64*)(ws + W_QG) + (kvh * 4 + (split & 3)) * 512 + (split >> 2) * 64 + lane, tag | (u64)((qs + 0x80u) >> 8), __ATOMIC_RELAXED, __HIP_MEMORY_SCOPE_AGENT);
    STAMP(1);
    { const u64 tp = rt(); _Pragma("nounroll") while (rt() - tp < QD) __builtin_amdgcn_s_sleep(2); }
    STAMP(12);
    {
      u32x4 gq[16];
      _Pragma("nounroll") for (int sp = 0; sp < SPIN_CAP; ++sp) { CFENCE();
#pragma unroll
        for (int k = 0; k < 16; ++k) gq[k] = __builtin_amdgcn_raw_buffer_load_b128(rs, W_QG + kvh * 16384 + k * 1024 + lane * 16, 0, AUX_SC1);
        unsigned bad = 0;
#pragma unroll
        for (int k = 0; k < 16; ++k) bad |= (((gq[k][0] ^ tlo) | (gq[k][2] ^ tlo)) & 0xff000000u) | (gq[k][1] ^ thi) | (gq[k][3] ^ thi);
        if (__all(bad == 0u)) break;
        __builtin_amdgcn_s_sleep(QSLEEP);
      }
#pragma unroll
      for (int k = 0; k < 4; ++k) {
        const float lo = (__uint_as_float(gq[k][0] << 8) + __uint_as_float(gq[4 + k][0] << 8)) + (__uint_as_float(gq[8 + k][0] << 8) + __uint_as_float(gq[12 + k][0] << 8));
        const float hi = (__uint_as_float(gq[k][2] << 8) + __uint_as_float(gq[4 + k][2] << 8)) + (__uint_as_float(gq[8 + k][2] << 8) + __uint_as_float(gq[12 + k][2] << 8));
        *(LAS unsigned*)(L8 + L_QS + k * 256 + lane * 4) = pk_bf16(lo, hi);
      }
      LDS_WAIT();
      CTRL[C_QREADY] = 1u;
    }
    STAMP(2);
    {
      const u16* wop = wo + (size_t)(bhead * DH + (lane >> 3)) * D + bcg * 64 + 8 * (lane & 7);
#pragma unroll
      for (int i = 0; i < 16; ++i) __builtin_amdgcn_global_load_lds((const unsigned*)(wop + i * 8 * D), (unsigned*)(lds + L_WO + i * 1024), 16, 0, 2);
    }
    LSPIN(CTRL[C_KL] < (unsigned)(NT / 2));
    _Pragma("nounroll") for (int sp = 0; sp < SPIN_CAP; ++sp) { CFENCE();
      unsigned bad = 0;
#pragma unroll
      for (int k = 0; k < 4; ++k) {
        const u32x2 t = __builtin_amdgcn_raw_buffer_load_b64(rs, W_QG + (lane + 64 * k) * 512, 0, AUX_SC1);
        bad |= ((t[0] ^ tlo) & 0xff000000u) | (t[1] ^ thi);
      }
      if (__all(bad == 0u)) break;
      __builtin_amdgcn_s_sleep(8);
    }
    if (b == 0 && lane == 0) __hip_atomic_store((GAS u64*)(ws + W_EPOCH), epoch + 1, __ATOMIC_RELAXED, __HIP_MEMORY_SCOPE_AGENT);
    asm volatile("s_waitcnt vmcnt(0)" ::: "memory");
    STAMP(11);
  } else {
    BAR();
  }

  const int tw = wave & 3;
  unsigned pw[7], pz[7], xr = 0, pbb = 0;
  if (wave >= 4) {
    const int t = tid - 256;
#pragma unroll
    for (int i = 0; i < 7; ++i) { pw[i] = (unsigned)w1[t + 256 * i] | ((unsigned)b1[t + 256 * i] << 16); pz[i] = w2[t + 256 * i]; }
    xr = *(const GAS unsigned*)(x + 128 * tw + 4 * (lane & 31) + 2 * (lane >> 5));
    pbb = *(const GAS u16*)b2;
  } else {
#pragma unroll
    for (int i = 0; i < 7; ++i) { pw[i] = 0; pz[i] = 0; }
  }
  if (wave != HELPER) {
    f32x4 o[8];
#pragma unroll
    for (int d = 0; d < 8; ++d) o[d] = f32x4{0.f, 0.f, 0.f, 0.f};
    float m = -INFINITY, l = 0.f;
    u32x4 qf[4];
#pragma unroll
    for (int s = 0; s < 4; ++s) qf[s] = u32x4{0u, 0u, 0u, 0u};
    constexpr float SCALE = 0.08838834764831845f;
#define TICKET() __builtin_amdgcn_readfirstlane(lane == 0 ? __hip_atomic_fetch_add((LAS unsigned*)(L8 + L_CTRL) + C_TICKET, 1u, __ATOMIC_RELAXED, __HIP_MEMORY_SCOPE_WORKGROUP) : 0u)
    const LAS unsigned char* ring = L8 + L_RING + lane * 16;
    unsigned q = TICKET();
    for (int it = 0;; ++it) {
      u32x4 tk[8], tv[8];
      const int s = q & (NSLOT - 1);
      const LAS unsigned char* sp = ring + s * SLOT;
      unsigned ntot;
#define WAIT_LANDED(idx) do { int sp_ = 0; _Pragma("nounroll") for (;;) { const u32x4 cw_ = *(const volatile LAS u32x4*)(L8 + L_CTRL); \
        ntot = __builtin_amdgcn_readfirstlane(cw_[C_NTOT]); \
        if (__builtin_amdgcn_readfirstlane(cw_[idx]) > q || ntot <= q || ++sp_ > SPIN_CAP) break; __builtin_amdgcn_s_sleep(1); } \
        __builtin_amdgcn_fence(__ATOMIC_ACQUIRE, "workgroup"); } while (0)
#define READ_V() do { WAIT_LANDED(C_VL); \
      _Pragma("unroll") for (int j = 0; j < 8; ++j) tv[j] = *(const LAS u32x4*)(sp + 8192 + j * 1024); \
      LDS_WAIT(); CTRL[C_FREE + s] = q + 1; \
      asm volatile("" : "+v"(tv[0]), "+v"(tv[1]), "+v"(tv[2]), "+v"(tv[3]), "+v"(tv[4]), "+v"(tv[5]), "+v"(tv[6]), "+v"(tv[7]) :: "memory"); } while (0)
      WAIT_LANDED(C_KL);
      if (ntot <= q || q >= (unsigned)(NT + NPOOL)) break;
      if (wave < 4) STAMP(5);
#pragma unroll
      for (int j = 0; j < 8; ++j) tk[j] = *(const LAS u32x4*)(sp + j * 1024);
      if (it == 0) {
        READ_V();
        LSPIN(CTRL[C_QREADY] == 0u);
        if (c < 4) {
#pragma unroll
          for (int s2 = 0; s2 < 4; ++s2) qf[s2] = *(const LAS u32x4*)(L8 + L_QS + c * 256 + s2 * 64 + g * 16);
        }
        STAMP(3);
      }
      f32x4 s0 = {0.f, 0.f, 0.f, 0.f}, s1 = {0.f, 0.f, 0.f, 0.f};
#pragma unroll
      for (int k = 0; k < 4; ++k) { s0 = mfma16(tk[k], qf[k], s0); s1 = mfma16(tk[4 + k], qf[k], s1); }
      unsigned qn = 0x7fffffffu;
      if (wave < 4 || q < (unsigned)(NSTAT - RETIRE)) qn = TICKET();
      f32x2 sc[4];
      sc[0] = rbf2(rbf2(f32x2{s0[0], s0[1]}) * SCALE); sc[1] = rbf2(rbf2(f32x2{s0[2], s0[3]}) * SCALE);
      sc[2] = rbf2(rbf2(f32x2{s1[0], s1[1]}) * SCALE); sc[3] = rbf2(rbf2(f32x2{s1[2], s1[3]}) * SCALE);
      const float mx = xg_max2(vmax3(vmax3(sc[0][0], sc[0][1], sc[1][0]), vmax3(sc[1][1], sc[2][0], sc[2][1]), vmax(sc[3][0], sc[3][1])));
      if (__any(mx > m)) {
        const float mn = vmax(m, mx);
        const float alpha = __builtin_amdgcn_exp2f((m - mn) * LOG2E);
        m = mn; l *= alpha;
#pragma unroll
        for (int d = 0; d < 8; ++d) o[d] *= alpha;
      }
      const float ms = m * LOG2E;
      float p[8];
#pragma unroll
      for (int r = 0; r < 8; ++r) p[r] = __builtin_amdgcn_exp2f(fmaf(sc[r >> 1][r & 1], LOG2E, -ms));
      l += xg_sum(((p[0] + p[1]) + (p[2] + p[3])) + ((p[4] + p[5]) + (p[6] + p[7])));
      u32x4 pf = {pk_bf16(p[0], p[1]), pk_bf16(p[2], p[3]), pk_bf16(p[4], p[5]), pk_bf16(p[6], p[7])};
      asm volatile("" : "+v"(pf), "+v"(l));
      if (wave < 4) STAMP(6);
      if (it != 0) READ_V();
      if (wave < 4) STAMP(7);
#pragma unroll
      for (int d = 0; d < 8; ++d) {
        u32x4 a;
#pragma unroll
        for (int jj = 0; jj < 4; ++jj) a[jj] = __builtin_amdgcn_perm(tv[2 * jj + 1][d >> 1], tv[2 * jj][d >> 1], (d & 1) ? 0x07060302u : 0x05040100u);
        o[d] = mfma16(a, pf, o[d]);
      }
      if (qn >= ntot) break;
      q = qn;
    }
    if (c < 4) {
      const int ci = wave > HELPER ? wave - 2 : wave - 1;
      LAS float* rec = (LAS float*)(L8 + L_MRG) + (ci * 4 + c) * 132;
      if (g == 0) *(LAS f32x2*)(L8 + L_MML + c * 64 + ci * 8) = f32x2{m * LOG2E, l};
#pragma unroll
      for (int d = 0; d < 8; ++d) *(LAS f32x4*)(rec + d * 16 + g * 4) = o[d];
    }
    STAMP(4);
  }
  if (wave < 4) {
    BAR(); STAMP_OUT();
    return;
  }

  float w1f[7], b1f[7], w2f[7];
#pragma unroll
  for (int i = 0; i < 7; ++i) { w1f[i] = bf2f(pw[i]); b1f[i] = bfhi(pw[i]); w2f[i] = bf2f(pz[i]); asm volatile("" : "+v"(w1f[i]), "+v"(b1f[i]), "+v"(w2f[i])); }
  const int hb = lane >> 5;
  int a_rec = L_MRG + (hb * 12 + tw) * 528 + (lane & 31) * 16;
  int a_mml = L_MML + tw * 64 + hb * 24;
  int o_pa = W_PA + (((4 * kvh + tw) * 5 + (lane >> 3)) * NSPLIT + split) * 128 + (lane & 7) * 16;
  int o_bd = W_PA + (bhead * 5 + tw) * 4096 + lane * 16, o_bm = W_PA + (bhead * 5 + 4) * 4096 + lane * 16;
  int o_y = W_Y + (bhead * D + bcg * 64 + 16 * tw + 4 * g) * 4;
  int o_c = W_Y + (hb * D + 128 * tw + 4 * (lane & 31)) * 4;
  int a_ob = L_OB + (tw * 32 + 4 * (lane & 7) + (lane >> 4)) * 2;
  f32x2 xf = unpk(xr); float bbf = bf2f(pbb);
  asm volatile("" : "+v"(a_rec), "+v"(a_mml), "+v"(o_pa), "+v"(o_bd), "+v"(o_bm), "+v"(o_y), "+v"(o_c), "+v"(a_ob), "+v"(xf), "+v"(bbf));
  BAR();
  {
    const f32x2 m0 = *(const LAS f32x2*)(L8 + a_mml), m1 = *(const LAS f32x2*)(L8 + a_mml + 8), m2 = *(const LAS f32x2*)(L8 + a_mml + 16);
    const f32x4 r0 = *(const LAS f32x4*)(L8 + a_rec), r1 = *(const LAS f32x4*)(L8 + a_rec + 2112), r2 = *(const LAS f32x4*)(L8 + a_rec + 4224);
    float M = vmax3(m0[0], m1[0], m2[0]), t;
    asm("v_mov_b32 %1, %0\n\ts_nop 1\n\tv_permlane32_swap_b32 %0, %1\n\tv_max_f32 %0, %0, %1" : "+v"(M), "=&v"(t));
    const float e0 = __builtin_amdgcn_exp2f(m0[0] - M), e1 = __builtin_amdgcn_exp2f(m1[0] - M), e2 = __builtin_amdgcn_exp2f(m2[0] - M);
    const float Lp = e0 * m0[1] + e1 * m1[1] + e2 * m2[1];
    const f32x4 acc = r0 * e0 + r1 * e1 + r2 * e2;
    u32x4 ub;
    ub[0] = __float_as_uint(swap32_add(acc[0], 0.5f * M));
    ub[1] = __float_as_uint(swap32_add(acc[1], Lp));
    ub[2] = __float_as_uint(swap32_add(acc[2], Lp));
    ub[3] = __float_as_uint(swap32_add(acc[3], Lp));
#pragma unroll
    for (int k = 0; k < 4; ++k) ub[k] = min(ub[k], SENT - 1u);
    if (lane < 40) __builtin_amdgcn_raw_buffer_store_b128(ub, rs, o_pa, 0, AUX_SC1);
  }
  STAMP(5);
  u32x4 wof[4];
#pragma unroll
  for (int s = 0; s < 4; ++s) {
    const int a = L_WO + (64 * (g & 1) + 8 * (c >> 2) + 2 * s + (g >> 1)) * 128 + tw * 32 + (c & 3) * 8;
    const u32x2 lo = __builtin_bit_cast(u32x2, __builtin_amdgcn_ds_read_tr16_b64_v4i16((LAS s16x4*)(L8 + a)));
    const u32x2 hi = __builtin_bit_cast(u32x2, __builtin_amdgcn_ds_read_tr16_b64_v4i16((LAS s16x4*)(L8 + a + 32 * 128)));
    wof[s] = u32x4{lo[0], lo[1], hi[0], hi[1]};
  }
  if (BD) { const u64 tp = rt(); _Pragma("nounroll") while (rt() - tp < BD) {} }
  {
    u32x4 a[4]; u32x2 mlv[4];
#pragma unroll
    for (int k = 0; k < 4; ++k) { a[k] = u32x4{0u, 0u, 0u, 0u}; mlv[k] = u32x2{0u, 0u}; }
    bool ok = false;
    _Pragma("nounroll") for (int sp = 0; sp < SPIN_CAP; ++sp) { CFENCE();
      if (!ok) {
#pragma unroll
        for (int k = 0; k < 4; ++k) mlv[k] = __builtin_amdgcn_raw_buffer_load_b64(rs, o_bm + k * 1024, 0, AUX_SC1);
#pragma unroll
        for (int k = 0; k < 4; ++k) a[k] = __builtin_amdgcn_raw_buffer_load_b128(rs, o_bd + k * 1024, 0, AUX_SC1);
        const unsigned mm = max(max(max(mlv[0][0], mlv[0][1]), max(mlv[1][0], mlv[1][1])), max(max(mlv[2][0], mlv[2][1]), max(mlv[3][0], mlv[3][1])));
        ok = max(max(max(umax4(a[0]), umax4(a[1])), max(umax4(a[2]), umax4(a[3]))), mm) != SENT;
      }
      if (__all(ok)) break;
      if (PSLEEP) __builtin_amdgcn_s_sleep(PSLEEP);
    }
    STAMP(6);
#define MF(k, i) __uint_as_float(mlv[k][i])
    const float M = xg_max2(xor8_max(vmax(vmax3(MF(0, 0), MF(1, 0), MF(2, 0)), MF(3, 0))));
    const float w0 = __builtin_amdgcn_exp2f(MF(0, 0) - M), w1_ = __builtin_amdgcn_exp2f(MF(1, 0) - M), w2_ = __builtin_amdgcn_exp2f(MF(2, 0) - M), w3 = __builtin_amdgcn_exp2f(MF(3, 0) - M);
    float L = (w0 * MF(0, 1) + w1_ * MF(1, 1)) + (w2_ * MF(2, 1) + w3 * MF(3, 1));
    const f32x4 v = (__builtin_bit_cast(f32x4, a[0]) * w0 + __builtin_bit_cast(f32x4, a[1]) * w1_) + (__builtin_bit_cast(f32x4, a[2]) * w2_ + __builtin_bit_cast(f32x4, a[3]) * w3);
    L = swap32_add(L, L);
    const float u0 = swap32_add(v[0], v[2]), u1 = swap32_add(v[1], v[3]);
    L = swap16_add(L, L);
    float u = swap16_add(u0, u1);
    L += dpp<XOR8>(L); u += dpp<XOR8>(u);
    const float ov = u * __builtin_amdgcn_rcpf(L);
    *(LAS u16*)(L8 + a_ob) = (u16)pk_bf16(ov, ov);
  }
  BAR();
  {
    f32x4 y = {0.f, 0.f, 0.f, 0.f}, y2 = {0.f, 0.f, 0.f, 0.f};
    const LAS unsigned char* ob = L8 + L_OB + g * 16;
    const u32x4 f0 = *(const LAS u32x4*)ob, f1 = *(const LAS u32x4*)(ob + 64), f2 = *(const LAS u32x4*)(ob + 128), f3 = *(const LAS u32x4*)(ob + 192);
    y = mfma16(wof[0], f0, y); y2 = mfma16(wof[1], f1, y2); y = mfma16(wof[2], f2, y); y2 = mfma16(wof[3], f3, y2);
    u32x4 ub = __builtin_bit_cast(u32x4, y + y2);
#pragma unroll
    for (int k = 0; k < 4; ++k) ub[k] = min(ub[k], SENT - 1u);
    if (c == 0) __builtin_amdgcn_raw_buffer_store_b128(ub, rs, o_y, 0, AUX_SC1);
  }
  STAMP(7);
  if (CD) { const u64 tp = rt(); _Pragma("nounroll") while (rt() - tp < CD) {} }
  {
    f32x4 s4;
    _Pragma("nounroll") for (int sp = 0; sp < SPIN_CAP; ++sp) { CFENCE();
      u32x4 yv[16];
#pragma unroll
      for (int k = 0; k < 16; ++k) yv[k] = __builtin_amdgcn_raw_buffer_load_b128(rs, o_c, k * 4096, AUX_SC1);
#define YF(i) __builtin_bit_cast(f32x4, yv[i])
      s4 = (((YF(0) + YF(1)) + (YF(2) + YF(3))) + ((YF(4) + YF(5)) + (YF(6) + YF(7)))) + (((YF(8) + YF(9)) + (YF(10) + YF(11))) + ((YF(12) + YF(13)) + (YF(14) + YF(15))));
      if (!__any(__builtin_isunordered(s4[0], s4[1]) || __builtin_isunordered(s4[2], s4[3]))) break;
      unsigned mx = 0;
#pragma unroll
      for (int k = 0; k < 16; ++k) mx = max(mx, umax4(yv[k]));
      if (!__any(mx == SENT)) break;
    }
    STAMP(8);
    const f32x2 at = {swap32_add(s4[0], s4[2]), swap32_add(s4[1], s4[3])};
    const f32x2 h = rbf2(xf + rbf2(at));
    const float piv = rdfirst(h[0]);
    const f32x2 d = h - piv, dd = d * d;
    float S, Q;
    wave_sum2(d[0] + d[1], dd[0] + dd[1], S, Q);
    const float md = S * (1.0f / 128.f);
    if (lane == 0) *(LAS f32x2*)(L8 + L_STAT + tw * 8) = f32x2{piv + md, fmaf(-md, S, Q)};
    if (tw == (b >> 6) && lane == ((b >> 1) & 31) + 32 * (b & 1)) *(LAS f32x2*)(L8 + L_STAT + 32) = h;
  }
  BAR();
  const f32x4 sa = *(const LAS f32x4*)(L8 + L_STAT), sb = *(const LAS f32x4*)(L8 + L_STAT + 16);
  const f32x2 hh = *(const LAS f32x2*)(L8 + L_STAT + 32);
  {
    const float mean = 0.25f * ((sa[0] + sa[2]) + (sb[0] + sb[2]));
    const f32x2 e0 = f32x2{sa[0], sa[2]} - mean, e1 = f32x2{sb[0], sb[2]} - mean, ee = e0 * e0 + e1 * e1;
    const float var = fmaxf(fmaf(ee[0] + ee[1], 0.25f, ((sa[1] + sa[3]) + (sb[1] + sb[3])) * (1.0f / D)), 0.f);
    const float mb = rbf(mean), rden = ln_rden(var);
    const float mb2 = rbf((mean - mb) * rden), rden2 = ln_rden(var * rden * rden);
    const f32x2 tt = ln_apply2(ln_apply2(hh, mb, rden), mb2, rden2);
    f32x2 a0 = {0.f, 0.f}, a1 = {0.f, 0.f};
#pragma unroll
    for (int i = 0; i < 7; ++i) { const f32x2 gl = gelu2(tt * w1f[i] + b1f[i]) * w2f[i]; if (i & 1) a1 += gl; else a0 += gl; }
    a0 += a1;
    float s0, s1;
    wave_sum2(a0[0], a0[1], s0, s1);
    if (lane == 0) *(LAS f32x2*)(L8 + L_RED + tw * 8) = f32x2{s0, s1};
  }
  BAR();
  if (wave == HELPER) {
    const f32x4 ra = *(const LAS f32x4*)(L8 + L_RED), rb = *(const LAS f32x4*)(L8 + L_RED + 16);
    const f32x2 y = {(ra[0] + ra[2]) + (rb[0] + rb[2]), (ra[1] + ra[3]) + (rb[1] + rb[3])};
    const f32x2 r = hh + rbf2(rbf2(y) + bbf);
    if (lane == 0) *(GAS unsigned*)(out + 2 * b) = pk_bf16(r[0], r[1]);
  }
  STAMP_OUT();
}

extern "C" void kernel_launch(void* const* d_in, const int* in_sizes, int n_in, void* d_out, int out_size, void* d_ws, size_t ws_size, hipStream_t stream) {
  (void)hipFuncSetAttribute((const void*)k_fused, hipFuncAttributeMaxDynamicSharedMemorySize, L_END);
  hipLaunchKernelGGL(k_fused, dim3(NWG), dim3(512), L_END, stream, (const u16*)d_in[0], (const u16*)d_in[1], (const u16*)d_in[2], (const u16*)d_in[3],
                     (const u16*)d_in[4], (const u16*)d_in[6], (const u16*)d_in[7], (const u16*)d_in[8], (const u16*)d_in[9], (u16*)d_out, (unsigned char*)d_ws);
}
```

```cpp
#include <hip/hip_runtime.h>
#include <hip/hip_bf16.h>
#include <stdint.h>

#define GAS __attribute__((address_space(1)))
#define LAS __attribute__((address_space(3)))
typedef unsigned u32x4 __attribute__((ext_vector_type(4)));
typedef unsigned u32x2 __attribute__((ext_vector_type(2)));
typedef float f32x4 __attribute__((ext_vector_type(4)));
typedef float f32x2 __attribute__((ext_vector_type(2)));
typedef short bf16x8 __attribute__((ext_vector_type(8)));
typedef short s16x4 __attribute__((ext_vector_type(4)));
typedef __bf16 bf16x2 __attribute__((ext_vector_type(2)));
typedef unsigned short u16;
typedef unsigned long long u64;

#define KPOL ""
#define VPOL " nt"
#define STAMPS 0
#define INFL 24
#define QD 100
#define RETIRE 8
#define PSLEEP 1
#define QSLEEP 4
#define CD 50
#define BD 0
#define XMAP 0
#define NSTAT 60

constexpr int D = 512, HQ = 32, SK = 65536, DH = 128, FF = 1792;
constexpr int NWG = 256, NSPLIT = 32;
constexpr int NT = 64;
constexpr int NSLOT = 8, SLOT = 16384;
constexpr int NC = 6;
constexpr int HELPER = 5;
constexpr int L_RING = 0;
constexpr int L_WO = NSLOT * SLOT;
constexpr int L_CTRL = L_WO + 16384;
constexpr int C_KL = 0, C_VL = 1, C_NTOT = 2, C_TICKET = 4, C_QCNT = 5, C_QREADY = 6, C_FREE = 16;
constexpr int L_QS = L_CTRL + 256;
constexpr int L_QP = L_QS + 1024;
constexpr int L_MRG = L_QP + 1024;
constexpr int L_MML = L_MRG + NC * 4 * 528;
constexpr int NPOOL = NSPLIT * (NT - NSTAT);
constexpr int L_OB = L_MML + 256;
constexpr int L_STAT = L_OB + 256;
constexpr int L_RED = L_STAT + 64;
constexpr int L_ST = L_RED + 64;
constexpr int L_END = L_ST + (STAMPS ? 512 : 0);
static_assert(L_END <= 160 * 1024, "LDS");
constexpr int PREC = 640;
constexpr int W_EPOCH = 0, W_POOL = 4096, W_Y = 65536, W_QG = 131072, W_PA = 262144, W_END = W_PA + HQ * NSPLIT * PREC;
constexpr int W_STAMP = 1 << 20;
constexpr unsigned SENT = 0xFFFFFFFFu;
constexpr int AUX_SC1 = 16;
constexpr float LOG2E = 1.4426950408889634f;
constexpr int SPIN_CAP = 1 << 18;

__device__ __forceinline__ float bf2f(unsigned b) { return __builtin_bit_cast(float, b << 16); }
__device__ __forceinline__ float bfhi(unsigned b) { return __builtin_bit_cast(float, b & 0xffff0000u); }
__device__ __forceinline__ f32x2 unpk(unsigned u) { return f32x2{bf2f(u), bfhi(u)}; }
__device__ __forceinline__ unsigned pk_bf16(float lo, float hi) {
  f32x2 v = {lo, hi};
  return __builtin_bit_cast(unsigned, __builtin_convertvector(v, bf16x2));
}
__device__ __forceinline__ float rbf(float x) { return bf2f(pk_bf16(x, x)); }
__device__ __forceinline__ f32x2 rbf2(f32x2 v) { return unpk(pk_bf16(v[0], v[1])); }
__device__ __forceinline__ f32x4 mfma16(u32x4 a, u32x4 b, f32x4 c) {
  return __builtin_amdgcn_mfma_f32_16x16x32_bf16(__builtin_bit_cast(bf16x8, a), __builtin_bit_cast(bf16x8, b), c, 0, 0, 0);
}
template <int CTRL> __device__ __forceinline__ float dpp(float x) {
  return __builtin_bit_cast(float, __builtin_amdgcn_mov_dpp(__builtin_bit_cast(int, x), CTRL, 0xf, 0xf, true));
}
constexpr int XOR1 = 0xB1, XOR2 = 0x4E, XOR7 = 0x141, XOR8 = 0x128;
__device__ __forceinline__ float swap16_add(float a, float b) {
  auto s = __builtin_amdgcn_permlane16_swap(__float_as_uint(a), __float_as_uint(b), false, false);
  return __uint_as_float(s[0]) + __uint_as_float(s[1]);
}
__device__ __forceinline__ float swap32_add(float a, float b) {
  auto s = __builtin_amdgcn_permlane32_swap(__float_as_uint(a), __float_as_uint(b), false, false);
  return __uint_as_float(s[0]) + __uint_as_float(s[1]);
}
__device__ __forceinline__ float xg_max(float x) {
  auto s = __builtin_amdgcn_permlane16_swap(__float_as_uint(x), __float_as_uint(x), false, false);
  x = fmaxf(__uint_as_float(s[0]), __uint_as_float(s[1]));
  auto t = __builtin_amdgcn_permlane32_swap(__float_as_uint(x), __float_as_uint(x), false, false);
  return fmaxf(__uint_as_float(t[0]), __uint_as_float(t[1]));
}
__device__ __forceinline__ float vmax(float a, float b) { float r; asm("v_max_f32 %0, %1, %2" : "=v"(r) : "v"(a), "v"(b)); return r; }
__device__ __forceinline__ float vmax3(float a, float b, float c) { float r; asm("v_max3_f32 %0, %1, %2, %3" : "=v"(r) : "v"(a), "v"(b), "v"(c)); return r; }
__device__ __forceinline__ float xg_max2(float x) {
  float t;
  asm("v_mov_b32 %1, %0\n\ts_nop 1\n\tv_permlane16_swap_b32 %0, %1\n\tv_max_f32 %0, %0, %1\n\tv_mov_b32 %1, %0\n\ts_nop 1\n\tv_permlane32_swap_b32 %0, %1\n\tv_max_f32 %0, %0, %1"
      : "+v"(x), "=&v"(t));
  return x;
}
__device__ __forceinline__ float xor8_max(float x) {
  asm("s_nop 1\n\tv_max_f32_dpp %0, %0, %0 row_ror:8 row_mask:0xf bank_mask:0xf" : "+v"(x));
  return x;
}
__device__ __forceinline__ float xg_sum(float x) { x = swap16_add(x, x); return swap32_add(x, x); }
__device__ __forceinline__ float rdlane(float v, int l) { return __builtin_bit_cast(float, __builtin_amdgcn_readlane(__builtin_bit_cast(int, v), l)); }
__device__ __forceinline__ float rdfirst(float v) { return __builtin_bit_cast(float, __builtin_amdgcn_readfirstlane(__builtin_bit_cast(int, v))); }
__device__ __forceinline__ void wave_sum2(float a, float b, float& sa, float& sb) {
  float v = swap32_add(a, b);
  v += dpp<XOR1>(v); v += dpp<XOR2>(v); v += dpp<XOR7>(v); v += dpp<XOR8>(v);
  v = swap16_add(v, v);
  sa = rdlane(v, 0); sb = rdlane(v, 32);
}
__device__ __forceinline__ float ln_rden(float var) { return __builtin_amdgcn_rcpf(rbf(__builtin_amdgcn_sqrtf(rbf(rbf(var) + 1e-5f)))); }
__device__ __forceinline__ f32x2 ln_apply2(f32x2 v, float mb, float rden) { return rbf2(rbf2(v - mb) * rden); }
__device__ __forceinline__ f32x2 gelu2(f32x2 u) {
  constexpr float C0 = 0.7978845608028654f * 2.0f * LOG2E, C1 = C0 * 0.044715f;
  const f32x2 z = u * (u * u * C1 + C0);
  const f32x2 e = {__builtin_amdgcn_exp2f(z[0]), __builtin_amdgcn_exp2f(z[1])};
  const f32x2 e1 = e + 1.0f;
  const f32x2 r = {__builtin_amdgcn_rcpf(e1[0]), __builtin_amdgcn_rcpf(e1[1])};
  return u - u * r;
}
__device__ __forceinline__ unsigned umax4(u32x4 v) { return max(max(v[0], v[1]), max(v[2], v[3])); }
__device__ __forceinline__ u64 rt() { return __builtin_amdgcn_s_memrealtime(); }
#define LDS_WAIT() asm volatile("s_waitcnt lgkmcnt(0)" ::: "memory")
#define CFENCE() asm volatile("" ::: "memory")
#define BAR() do { __builtin_amdgcn_sched_barrier(0); LDS_WAIT(); __builtin_amdgcn_s_barrier(); CFENCE(); __builtin_amdgcn_sched_barrier(0); } while (0)
#define LSPIN(cond) do { int sp_ = 0; _Pragma("nounroll") while (cond) { __builtin_amdgcn_s_sleep(1); if (++sp_ > SPIN_CAP) break; } __builtin_amdgcn_fence(__ATOMIC_ACQUIRE, "workgroup"); } while (0)
#define STAMP(i) do {} while (0)
#define STAMPC(i) do {} while (0)
#define STAMP_OUT() do {} while (0)
#define STR2(x) #x
#define STR(x) STR2(x)

__global__ void __launch_bounds__(512, 2)
k_fused(const u16* __restrict__ x, const u16* __restrict__ K, const u16* __restrict__ V, const u16* __restrict__ wq, const u16* __restrict__ wo,
        const u16* __restrict__ w1, const u16* __restrict__ b1, const u16* __restrict__ w2, const u16* __restrict__ b2, u16* __restrict__ out,
        unsigned char* __restrict__ ws) {
  extern __shared__ __attribute__((aligned(16))) unsigned char lds[];
  LAS unsigned char* L8 = (LAS unsigned char*)lds;
  volatile LAS unsigned* CTRL = (volatile LAS unsigned*)(L8 + L_CTRL);
  const int tid = threadIdx.x, lane = tid & 63, wave = __builtin_amdgcn_readfirstlane(tid >> 6), b = blockIdx.x;
  const int g = lane >> 4, c = lane & 15;
  const int kvh = XMAP ? (b & 7) : (b >> 5), split = XMAP ? (b >> 3) : (b & 31);
  const int bhead = 4 * (b & 7) + (b >> 6), bcg = (b >> 3) & 7;
  const __amdgpu_buffer_rsrc_t rs = __builtin_amdgcn_make_buffer_rsrc((void*)ws, 0, W_END, 0x00020000);
  {
    const u64 kb = (u64)K + (u64)kvh * (SK * DH * 2) - 3072, vb = (u64)V + (u64)kvh * (SK * DH * 2), pool = (u64)ws + W_POOL + kvh * 256;
    const unsigned vk0 = (4 * (c & 3) + (c >> 2)) * 256 + g * 16 + 3072;
    const unsigned vinit = lane == C_NTOT ? 0x7fffffffu : 0u;
    unsigned sq, st, st1, st2, spk, spv, sa, sb, sm0;
    unsigned vt, va, vc;
#define GL(v, off) "global_load_lds_dwordx4 %[" v "], s[90:91] offset:" off KPOL "\n\t"
#define GV(v, off) "global_load_lds_dwordx4 %[" v "], s[90:91] offset:" off VPOL "\n\t"
#define PUB(reg, off) "v_mov_b32 %[vt], %[" reg "]\n\tds_write_b32 %[vc], %[vt] offset:" off "\n\t"
#define LST(i)
#define LSTQ(qq, i)
    asm volatile(
        "s_cmp_lg_u32 %[wave], 0\n\t"
        "s_cbranch_scc1 Lld_end\n\t"
        "s_mov_b32 %[sq], 0\n\t"
        "s_mov_b32 %[spk], 0\n\t"
        "s_mov_b32 %[spv], 0\n\t"
        "v_mov_b32 %[vc], %[ctrl]\n\t"
        "v_lshrrev_b32 %[vt], 2, %[vv]\n\t"
        "v_add_u32 %[vt], %[vc], %[vt]\n\t"
        "ds_write_b32 %[vt], %[vinit]\n\t"
        LST("12")
        "Lld_loop:\n\t"
        "s_cmp_lt_u32 %[sq], %[nstat]-2\n\t"
        "s_cbranch_scc1 Lld_noclaim\n\t"
        "s_add_u32 %[sa], %[sq], 2\n\t"
        "s_lshl_b64 exec, 1, %[sa]\n\t"
        "v_mov_b32 %[vt], 0\n\t"
        "v_mov_b32 %[va], 1\n\t"
        "global_atomic_add %[va], %[vt], %[va], %[pool] sc0\n\t"
        "s_mov_b64 exec, -1\n\t"
        "Lld_noclaim:\n\t"
        "s_add_u32 %[st], %[tile0], %[sq]\n\t"
        "s_cmp_lt_u32 %[sq], %[nstat]\n\t"
        "s_cbranch_scc1 Lld_havetile\n\t"
        "v_readlane_b32 %[sa], %[va], %[sq]\n\t"
        "s_nop 0\n\t"
        "s_cmp_ge_u32 %[sa], %[npool]\n\t"
        "s_cbranch_scc1 Lld_tail\n\t"
        "s_and_b32 %[st], %[sa], 31\n\t"
        "s_lshl_b32 %[st], %[st], 6\n\t"
        "s_lshr_b32 %[sa], %[sa], 5\n\t"
        "s_add_u32 %[st], %[st], %[sa]\n\t"
        "s_add_u32 %[st], %[st], %[nstat]\n\t"
        "Lld_havetile:\n\t"
        "s_cmp_lt_u32 %[sq], 8\n\t"
        "s_cbranch_scc1 Lld_free\n\t"
        "s_and_b32 %[sa], %[sq], 7\n\t"
        "s_lshl_b32 %[sa], %[sa], 2\n\t"
        "s_mov_b32 %[sb], %[cap]\n\t"
        "Lld_spin:\n\t"
        "v_add_u32 %[vt], %[sa], %[vc]\n\t"
        "ds_read_b32 %[vt], %[vt] offset:64\n\t"
        "s_waitcnt lgkmcnt(0)\n\t"
        "v_readfirstlane_b32 %[sm0], %[vt]\n\t"
        "s_nop 0\n\t"
        "s_add_u32 %[sm0], %[sm0], 7\n\t"
        "s_cmp_ge_u32 %[sm0], %[sq]\n\t"
        "s_cbranch_scc1 Lld_free\n\t"
        "s_waitcnt vmcnt(0)\n\t"
        "s_mov_b32 %[spk], %[sq]\n\t"
        "s_sub_u32 %[spv], %[sq], 2\n\t"
        PUB("spk", "0") PUB("spv", "4")
        "s_sleep 1\n\t"
        "s_sub_u32 %[sb], %[sb], 1\n\t"
        "s_cmp_lg_u32 %[sb], 0\n\t"
        "s_cbranch_scc1 Lld_spin\n\t"
        "Lld_free:\n\t"
        "s_lshl_b32 %[sa], %[st], 13\n\t"
        "s_add_u32 s90, %[kbl], %[sa]\n\t"
        "s_addc_u32 s91, %[kbh], 0\n\t"
        "s_and_b32 %[sa], %[sq], 7\n\t"
        "s_lshl_b32 m0, %[sa], 14\n\t"
        "s_nop 0\n\t"
        GL("vk0", "0") GL("vk1", "1024") GL("vk2", "2048") GL("vk3", "3072")
        "s_addk_i32 m0, 0x1000\n\t"
        "s_add_u32 s90, s90, 0x1000\n\t"
        "s_addc_u32 s91, s91, 0\n\t"
        GL("vk0", "0") GL("vk1", "1024") GL("vk2", "2048") GL("vk3", "3072")
        "s_cmp_lt_u32 %[sq], 2\n\t"
        "s_cbranch_scc1 Lld_next\n\t"
        "s_waitcnt vmcnt(24)\n\t"
        "s_sub_i32 %[sa], %[sq], 3\n\t"
        "s_max_i32 %[spv], %[spv], %[sa]\n\t"
        PUB("spv", "4")
        "s_lshl_b32 %[sa], %[st2], 13\n\t"
        "s_add_u32 s90, %[vbl], %[sa]\n\t"
        "s_addc_u32 s91, %[vbh], 0\n\t"
        "s_sub_u32 %[sa], %[sq], 2\n\t"
        "s_and_b32 %[sa], %[sa], 7\n\t"
        "s_lshl_b32 %[sa], %[sa], 14\n\t"
        "s_add_u32 m0, %[sa], 0x2000\n\t"
        "s_nop 0\n\t"
        GV("vv", "0") GV("vv", "1024") GV("vv", "2048") GV("vv", "3072")
        "s_addk_i32 m0, 0x1000\n\t"
        "s_add_u32 s90, s90, 0x1000\n\t"
        "s_addc_u32 s91, s91, 0\n\t"
        GV("vv", "0") GV("vv", "1024") GV("vv", "2048") GV("vv", "3072")
        "s_cmp_lg_u32 %[sq], 2\n\t"
        "s_cbranch_scc1 Lld_nobar\n\t"
        LST("13")
        "s_waitcnt lgkmcnt(0)\n\t"
        "s_barrier\n\t"
        "s_branch Lld_next\n\t"
        "Lld_nobar:\n\t"
        "s_waitcnt vmcnt(24)\n\t"
        "s_max_i32 %[spk], %[spk], %[sq]\n\t"
        PUB("spk", "0")
        LSTQ("4", "2") LSTQ("8", "3") LSTQ("16", "4") LSTQ("32", "10") LSTQ("48", "11")
        "Lld_next:\n\t"
        "s_mov_b32 %[st2], %[st1]\n\t"
        "s_mov_b32 %[st1], %[st]\n\t"
        "s_add_u32 %[sq], %[sq], 1\n\t"
        "s_branch Lld_loop\n\t"
        "Lld_tail:\n\t"
        PUB("sq", "8")
        "s_mov_b32 %[sb], 2\n\t"
        "Lld_tail2:\n\t"
        "s_lshl_b32 %[sa], %[st2], 13\n\t"
        "s_add_u32 s90, %[vbl], %[sa]\n\t"
        "s_addc_u32 s91, %[vbh], 0\n\t"
        "s_sub_u32 %[sa], %[sq], %[sb]\n\t"
        "s_and_b32 %[sa], %[sa], 7\n\t"
        "s_lshl_b32 %[sa], %[sa], 14\n\t"
        "s_add_u32 m0, %[sa], 0x2000\n\t"
        "s_nop 0\n\t"
        GV("vv", "0") GV("vv", "1024") GV("vv", "2048") GV("vv", "3072")
        "s_addk_i32 m0, 0x1000\n\t"
        "s_add_u32 s90, s90, 0x1000\n\t"
        "s_addc_u32 s91, s91, 0\n\t"
        GV("vv", "0") GV("vv", "1024") GV("vv", "2048") GV("vv", "3072")
        "s_mov_b32 %[st2], %[st1]\n\t"
        "s_sub_u32 %[sb], %[sb], 1\n\t"
        "s_cmp_lg_u32 %[sb], 0\n\t"
        "s_cbranch_scc1 Lld_tail2\n\t"
        "s_waitcnt vmcnt(32)\n\t"
        "s_sub_i32 %[sa], %[sq], 3\n\t"
        "s_max_i32 %[spv], %[spv], %[sa]\n\t"
        PUB("spv", "4")
        "s_waitcnt vmcnt(24)\n\t"
        PUB("sq", "0")
        LST("5")
        "s_sub_u32 %[spv], %[sq], 2\n\t"
        "s_waitcnt vmcnt(16)\n\t"
        PUB("spv", "4")
        "s_add_u32 %[spv], %[spv], 1\n\t"
        "s_waitcnt vmcnt(8)\n\t"
        PUB("spv", "4")
        "s_waitcnt vmcnt(0)\n\t"
        PUB("sq", "4")
        LST("1")
        "s_waitcnt lgkmcnt(0)\n\t"
        "s_endpgm\n\t"
        "Lld_end:"
        : [sq] "=&s"(sq), [st] "=&s"(st), [st1] "=&s"(st1), [st2] "=&s"(st2), [spk] "=&s"(spk), [spv] "=&s"(spv), [sa] "=&s"(sa), [sb] "=&s"(sb), [sm0] "=&s"(sm0),
          [vt] "=&v"(vt), [va] "=&v"(va), [vc] "=&v"(vc)
        : [wave] "s"(wave), [kbl] "s"((unsigned)kb), [kbh] "s"((unsigned)(kb >> 32)), [vbl] "s"((unsigned)vb), [vbh] "s"((unsigned)(vb >> 32)), [pool] "s"(pool), [tile0] "s"(split * NT), [vk0] "v"(vk0), [vk1] "v"(vk0 + 64 - 1024), [vk2] "v"(vk0 + 128 - 2048),
          [vk3] "v"(vk0 + 192 - 3072), [vv] "v"(lane * 16), [vinit] "v"(vinit), [ctrl] "i"(L_CTRL), [nstat] "i"(NSTAT), [npool] "i"(NPOOL), [cap] "i"(SPIN_CAP), [st0] "i"(L_ST - L_CTRL),
          "s"(x), "s"(wq), "s"(wo), "s"(w1), "s"(b1), "s"(w2), "s"(b2), "s"(out)
        : "memory", "scc", "s90", "s91", "s92", "s93");
  }
  if (wave <= 4) {
    const u32x4 xv = *(const GAS u32x4*)(x + 8 * lane);
    const int row0 = (split & 3) * 128 + 32 * (wave - 1) + 4 * (lane >> 3);
    const u32x2 xo = *(const GAS u32x2*)(x + row0);
    u32x4 wqv[4];
    const u16* wp = wq + (size_t)row0 * (HQ * DH) + kvh * 512 + (split >> 2) * 64 + 8 * (lane & 7);
#pragma unroll
    for (int i = 0; i < 4; ++i) wqv[i] = __builtin_nontemporal_load((const GAS u32x4*)(wp + i * (HQ * DH)));
    BAR();
    const float piv = rdfirst(bf2f(xv[0]));
    const f32x2 d0 = unpk(xv[0]) - piv, d1 = unpk(xv[1]) - piv, d2 = unpk(xv[2]) - piv, d3 = unpk(xv[3]) - piv;
    const f32x2 s = (d0 + d1) + (d2 + d3), qq = (d0 * d0 + d1 * d1) + (d2 * d2 + d3 * d3);
    float S, Q;
    wave_sum2(s[0] + s[1], qq[0] + qq[1], S, Q);
    const float md = S * (1.0f / D);
    const float mb = rbf(piv + md), rden = ln_rden(fmaxf(fmaf(-md, md, Q * (1.0f / D)), 0.f));
    const f32x2 xa = ln_apply2(unpk(xo[0]), mb, rden), xb = ln_apply2(unpk(xo[1]), mb, rden);
    STAMP(10);
    f32x2 a2[4];
#pragma unroll
    for (int j = 0; j < 4; ++j) a2[j] = (unpk(wqv[0][j]) * xa[0] + unpk(wqv[1][j]) * xa[1]) + (unpk(wqv[2][j]) * xb[0] + unpk(wqv[3][j]) * xb[1]);
    float a[8];
#pragma unroll
    for (int j = 0; j < 8; ++j) a[j] = a2[j >> 1][j & 1];
#pragma unroll
    for (int j = 0; j < 4; ++j) a[j] = swap32_add(a[j], a[j + 4]);
#pragma unroll
    for (int j = 0; j < 2; ++j) a[j] = swap16_add(a[j], a[j + 2]);
    { const float keep = (lane & 8) ? a[1] : a[0], send = (lane & 8) ? a[0] : a[1]; a[0] = keep + dpp<XOR8>(send); }
    ((LAS float*)(L8 + L_QP))[(wave - 1) * 64 + 8 * (lane & 7) + (lane >> 3)] = a[0];
    LDS_WAIT();
    if (lane == 0) __hip_atomic_fetch_add((LAS unsigned*)(L8 + L_CTRL) + C_QCNT, 1u, __ATOMIC_RELAXED, __HIP_MEMORY_SCOPE_WORKGROUP);
    STAMP(1);
  } else if (wave == HELPER) {
    const u64 epoch = __hip_atomic_load((GAS u64*)(ws + W_EPOCH), __ATOMIC_RELAXED, __HIP_MEMORY_SCOPE_AGENT);
    const u32x4 sv = {SENT, SENT, SENT, SENT};
    if (lane < 40) {
#pragma unroll
      for (int h = 0; h < 4; ++h) __builtin_amdgcn_raw_buffer_store_b128(sv, rs, W_PA + (((4 * kvh + h) * 5 + (lane >> 3)) * NSPLIT + split) * 128 + (lane & 7) * 16, 0, AUX_SC1);
    }
    if (lane < 16) __builtin_amdgcn_raw_buffer_store_b128(sv, rs, W_Y + (bhead * D + bcg * 64) * 4 + lane * 16, 0, AUX_SC1);
    if (split == 0 && lane == 0) __builtin_amdgcn_raw_buffer_store_b32(0u, rs, W_POOL + kvh * 256, 0, AUX_SC1);
    BAR();
    asm volatile("s_waitcnt vmcnt(0)" ::: "memory");
    STAMP(10);
    const u64 tag = (epoch + 1) << 24;
    const unsigned tlo = (unsigned)tag, thi = (unsigned)(tag >> 32);
    LSPIN(CTRL[C_QCNT] < 4u);
    const LAS float* qp = (const LAS float*)(L8 + L_QP);
    const unsigned qs = __float_as_uint((qp[lane] + qp[64 + lane]) + (qp[128 + lane] + qp[192 + lane]));
    __hip_atomic_store((GAS u64*)(ws + W_QG) + (kvh * 4 + (split & 3)) * 512 + (split >> 2) * 64 + lane, tag | (u64)((qs + 0x80u) >> 8), __ATOMIC_RELAXED, __HIP_MEMORY_SCOPE_AGENT);
    STAMP(1);
    { const u64 tp = rt(); _Pragma("nounroll") while (rt() - tp < QD) __builtin_amdgcn_s_sleep(2); }
    STAMP(12);
    {
      u32x4 gq[16];
      _Pragma("nounroll") for (int sp = 0; sp < SPIN_CAP; ++sp) { CFENCE();
#pragma unroll
        for (int k = 0; k < 16; ++k) gq[k] = __builtin_amdgcn_raw_buffer_load_b128(rs, W_QG + kvh * 16384 + k * 1024 + lane * 16, 0, AUX_SC1);
        unsigned bad = 0;
#pragma unroll
        for (int k = 0; k < 16; ++k) bad |= (((gq[k][0] ^ tlo) | (gq[k][2] ^ tlo)) & 0xff000000u) | (gq[k][1] ^ thi) | (gq[k][3] ^ thi);
        if (__all(bad == 0u)) break;
        __builtin_amdgcn_s_sleep(QSLEEP);
      }
#pragma unroll
      for (int k = 0; k < 4; ++k) {
        const float lo = (__uint_as_float(gq[k][0] << 8) + __uint_as_float(gq[4 + k][0] << 8)) + (__uint_as_float(gq[8 + k][0] << 8) + __uint_as_float(gq[12 + k][0] << 8));
        const float hi = (__uint_as_float(gq[k][2] << 8) + __uint_as_float(gq[4 + k][2] << 8)) + (__uint_as_float(gq[8 + k][2] << 8) + __uint_as_float(gq[12 + k][2] << 8));
        *(LAS unsigned*)(L8 + L_QS + k * 256 + lane * 4) = pk_bf16(lo, hi);
      }
      LDS_WAIT();
      CTRL[C_QREADY] = 1u;
    }
    STAMP(2);
    {
      const u16* wop = wo + (size_t)(bhead * DH + (lane >> 3)) * D + bcg * 64 + 8 * (lane & 7);
#pragma unroll
      for (int i = 0; i < 16; ++i) __builtin_amdgcn_global_load_lds((const unsigned*)(wop + i * 8 * D), (unsigned*)(lds + L_WO + i * 1024), 16, 0, 2);
    }
    LSPIN(CTRL[C_KL] < (unsigned)(NT / 2));
    _Pragma("nounroll") for (int sp = 0; sp < SPIN_CAP; ++sp) { CFENCE();
      unsigned bad = 0;
#pragma unroll
      for (int k = 0; k < 4; ++k) {
        const u32x2 t = __builtin_amdgcn_raw_buffer_load_b64(rs, W_QG + (lane + 64 * k) * 512, 0, AUX_SC1);
        bad |= ((t[0] ^ tlo) & 0xff000000u) | (t[1] ^ thi);
      }
      if (__all(bad == 0u)) break;
      __builtin_amdgcn_s_sleep(8);
    }
    if (b == 0 && lane == 0) __hip_atomic_store((GAS u64*)(ws + W_EPOCH), epoch + 1, __ATOMIC_RELAXED, __HIP_MEMORY_SCOPE_AGENT);
    asm volatile("s_waitcnt vmcnt(0)" ::: "memory");
    STAMP(11);
  } else {
    BAR();
  }

  const int tw = wave & 3;
  unsigned pw[7], pz[7], xr = 0, pbb = 0;
  if (wave >= 4) {
    const int t = tid - 256;
#pragma unroll
    for (int i = 0; i < 7; ++i) { pw[i] = (unsigned)w1[t + 256 * i] | ((unsigned)b1[t + 256 * i] << 16); pz[i] = w2[t + 256 * i]; }
    xr = *(const GAS unsigned*)(x + 128 * tw + 4 * (lane & 31) + 2 * (lane >> 5));
    pbb = *(const GAS u16*)b2;
  } else {
#pragma unroll
    for (int i = 0; i < 7; ++i) { pw[i] = 0; pz[i] = 0; }
  }
  if (wave != HELPER) {
    f32x4 o[8];
#pragma unroll
    for (int d = 0; d < 8; ++d) o[d] = f32x4{0.f, 0.f, 0.f, 0.f};
    float m = -INFINITY, l = 0.f;
    u32x4 qf[4];
#pragma unroll
    for (int s = 0; s < 4; ++s) qf[s] = u32x4{0u, 0u, 0u, 0u};
    constexpr float SCALE = 0.08838834764831845f;
#define TICKET() __builtin_amdgcn_readfirstlane(lane == 0 ? __hip_atomic_fetch_add((LAS unsigned*)(L8 + L_CTRL) + C_TICKET, 1u, __ATOMIC_RELAXED, __HIP_MEMORY_SCOPE_WORKGROUP) : 0u)
    const LAS unsigned char* ring = L8 + L_RING + lane * 16;
    unsigned q = TICKET();
    for (int it = 0;; ++it) {
      u32x4 tk[8], tv[8];
      const int s = q & (NSLOT - 1);
      const LAS unsigned char* sp = ring + s * SLOT;
      unsigned ntot;
#define WAIT_LANDED(idx) do { int sp_ = 0; _Pragma("nounroll") for (;;) { const u32x4 cw_ = *(const volatile LAS u32x4*)(L8 + L_CTRL); \
        ntot = __builtin_amdgcn_readfirstlane(cw_[C_NTOT]); \
        if (__builtin_amdgcn_readfirstlane(cw_[idx]) > q || ntot <= q || ++sp_ > SPIN_CAP) break; __builtin_amdgcn_s_sleep(1); } \
        __builtin_amdgcn_fence(__ATOMIC_ACQUIRE, "workgroup"); } while (0)
#define READ_V() do { WAIT_LANDED(C_VL); \
      _Pragma("unroll") for (int j = 0; j < 8; ++j) tv[j] = *(const LAS u32x4*)(sp + 8192 + j * 1024); \
      LDS_WAIT(); CTRL[C_FREE + s] = q + 1; \
      asm volatile("" : "+v"(tv[0]), "+v"(tv[1]), "+v"(tv[2]), "+v"(tv[3]), "+v"(tv[4]), "+v"(tv[5]), "+v"(tv[6]), "+v"(tv[7]) :: "memory"); } while (0)
      WAIT_LANDED(C_KL);
      if (ntot <= q || q >= (unsigned)(NT + NPOOL)) break;
      if (wave < 4) STAMP(5);
#pragma unroll
      for (int j = 0; j < 8; ++j) tk[j] = *(const LAS u32x4*)(sp + j * 1024);
      if (it == 0) {
        READ_V();
        LSPIN(CTRL[C_QREADY] == 0u);
        if (c < 4) {
#pragma unroll
          for (int s2 = 0; s2 < 4; ++s2) qf[s2] = *(const LAS u32x4*)(L8 + L_QS + c * 256 + s2 * 64 + g * 16);
        }
        STAMP(3);
      }
      f32x4 s0 = {0.f, 0.f, 0.f, 0.f}, s1 = {0.f, 0.f, 0.f, 0.f};
#pragma unroll
      for (int k = 0; k < 4; ++k) { s0 = mfma16(tk[k], qf[k], s0); s1 = mfma16(tk[4 + k], qf[k], s1); }
      unsigned qn = 0x7fffffffu;
      if (wave < 4 || q < (unsigned)(NSTAT - RETIRE)) qn = TICKET();
      f32x2 sc[4];
      sc[0] = rbf2(rbf2(f32x2{s0[0], s0[1]}) * SCALE); sc[1] = rbf2(rbf2(f32x2{s0[2], s0[3]}) * SCALE);
      sc[2] = rbf2(rbf2(f32x2{s1[0], s1[1]}) * SCALE); sc[3] = rbf2(rbf2(f32x2{s1[2], s1[3]}) * SCALE);
      const float mx = xg_max2(vmax3(vmax3(sc[0][0], sc[0][1], sc[1][0]), vmax3(sc[1][1], sc[2][0], sc[2][1]), vmax(sc[3][0], sc[3][1])));
      if (__any(mx > m)) {
        const float mn = vmax(m, mx);
        const float alpha = __builtin_amdgcn_exp2f((m - mn) * LOG2E);
        m = mn; l *= alpha;
#pragma unroll
        for (int d = 0; d < 8; ++d) o[d] *= alpha;
      }
      const float ms = m * LOG2E;
      float p[8];
#pragma unroll
      for (int r = 0; r < 8; ++r) p[r] = __builtin_amdgcn_exp2f(fmaf(sc[r >> 1][r & 1], LOG2E, -ms));
      l += xg_sum(((p[0] + p[1]) + (p[2] + p[3])) + ((p[4] + p[5]) + (p[6] + p[7])));
      u32x4 pf = {pk_bf16(p[0], p[1]), pk_bf16(p[2], p[3]), pk_bf16(p[4], p[5]), pk_bf16(p[6], p[7])};
      asm volatile("" : "+v"(pf), "+v"(l));
      if (wave < 4) STAMP(6);
      if (it != 0) READ_V();
      if (wave < 4) STAMP(7);
#pragma unroll
      for (int d = 0; d < 8; ++d) {
        u32x4 a;
#pragma unroll
        for (int jj = 0; jj < 4; ++jj) a[jj] = __builtin_amdgcn_perm(tv[2 * jj + 1][d >> 1], tv[2 * jj][d >> 1], (d & 1) ? 0x07060302u : 0x05040100u);
        o[d] = mfma16(a, pf, o[d]);
      }
      if (qn >= ntot) break;
      q = qn;
    }
    if (c < 4) {
      const int ci = wave > HELPER ? wave - 2 : wave - 1;
      LAS float* rec = (LAS float*)(L8 + L_MRG) + (ci * 4 + c) * 132;
      if (g == 0) *(LAS f32x2*)(L8 + L_MML + c * 64 + ci * 8) = f32x2{m * LOG2E, l};
#pragma unroll
      for (int d = 0; d < 8; ++d) *(LAS f32x4*)(rec + d * 16 + g * 4) = o[d];
    }
    STAMP(4);
  }
  if (wave < 4) {
    BAR(); STAMP_OUT();
    return;
  }

  float w1f[7], b1f[7], w2f[7];
#pragma unroll
  for (int i = 0; i < 7; ++i) { w1f[i] = bf2f(pw[i]); b1f[i] = bfhi(pw[i]); w2f[i] = bf2f(pz[i]); asm volatile("" : "+v"(w1f[i]), "+v"(b1f[i]), "+v"(w2f[i])); }
  const int hb = lane >> 5;
  int a_rec = L_MRG + (hb * 12 + tw) * 528 + (lane & 31) * 16;
  int a_mml = L_MML + tw * 64 + hb * 24;
  int o_pa = W_PA + (((4 * kvh + tw) * 5 + (lane >> 3)) * NSPLIT + split) * 128 + (lane & 7) * 16;
  int o_bd = W_PA + (bhead * 5 + tw) * 4096 + lane * 16, o_bm = W_PA + (bhead * 5 + 4) * 4096 + lane * 16;
  int o_y = W_Y + (bhead * D + bcg * 64 + 16 * tw + 4 * g) * 4;
  int o_c = W_Y + (hb * D + 128 * tw + 4 * (lane & 31)) * 4;
  int a_ob = L_OB + (tw * 32 + 4 * (lane & 7) + (lane >> 4)) * 2;
  f32x2 xf = unpk(xr); float bbf = bf2f(pbb);
  asm volatile("" : "+v"(a_rec), "+v"(a_mml), "+v"(o_pa), "+v"(o_bd), "+v"(o_bm), "+v"(o_y), "+v"(o_c), "+v"(a_ob), "+v"(xf), "+v"(bbf));
  BAR();
  {
    const f32x2 m0 = *(const LAS f32x2*)(L8 + a_mml), m1 = *(const LAS f32x2*)(L8 + a_mml + 8), m2 = *(const LAS f32x2*)(L8 + a_mml + 16);
    const f32x4 r0 = *(const LAS f32x4*)(L8 + a_rec), r1 = *(const LAS f32x4*)(L8 + a_rec + 2112), r2 = *(const LAS f32x4*)(L8 + a_rec + 4224);
    float M = vmax3(m0[0], m1[0], m2[0]), t;
    asm("v_mov_b32 %1, %0\n\ts_nop 1\n\tv_permlane32_swap_b32 %0, %1\n\tv_max_f32 %0, %0, %1" : "+v"(M), "=&v"(t));
    const float e0 = __builtin_amdgcn_exp2f(m0[0] - M), e1 = __builtin_amdgcn_exp2f(m1[0] - M), e2 = __builtin_amdgcn_exp2f(m2[0] - M);
    const float Lp = e0 * m0[1] + e1 * m1[1] + e2 * m2[1];
    const f32x4 acc = r0 * e0 + r1 * e1 + r2 * e2;
    u32x4 ub;
    ub[0] = __float_as_uint(swap32_add(acc[0], 0.5f * M));
    ub[1] = __float_as_uint(swap32_add(acc[1], Lp));
    ub[2] = __float_as_uint(swap32_add(acc[2], Lp));
    ub[3] = __float_as_uint(swap32_add(acc[3], Lp));
#pragma unroll
    for (int k = 0; k < 4; ++k) ub[k] = min(ub[k], SENT - 1u);
    if (lane < 40) __builtin_amdgcn_raw_buffer_store_b128(ub, rs, o_pa, 0, AUX_SC1);
  }
  STAMP(5);
  u32x4 wof[4];
#pragma unroll
  for (int s = 0; s < 4; ++s) {
    const int a = L_WO + (64 * (g & 1) + 8 * (c >> 2) + 2 * s + (g >> 1)) * 128 + tw * 32 + (c & 3) * 8;
    const u32x2 lo = __builtin_bit_cast(u32x2, __builtin_amdgcn_ds_read_tr16_b64_v4i16((LAS s16x4*)(L8 + a)));
    const u32x2 hi = __builtin_bit_cast(u32x2, __builtin_amdgcn_ds_read_tr16_b64_v4i16((LAS s16x4*)(L8 + a + 32 * 128)));
    wof[s] = u32x4{lo[0], lo[1], hi[0], hi[1]};
  }
  if (BD) { const u64 tp = rt(); _Pragma("nounroll") while (rt() - tp < BD) {} }
  {
    u32x4 a[4]; u32x2 mlv[4];
#pragma unroll
    for (int k = 0; k < 4; ++k) { a[k] = u32x4{0u, 0u, 0u, 0u}; mlv[k] = u32x2{0u, 0u}; }
    bool ok = false;
    _Pragma("nounroll") for (int sp = 0; sp < SPIN_CAP; ++sp) { CFENCE();
      if (!ok) {
#pragma unroll
        for (int k = 0; k < 4; ++k) mlv[k] = __builtin_amdgcn_raw_buffer_load_b64(rs, o_bm + k * 1024, 0, AUX_SC1);
#pragma unroll
        for (int k = 0; k < 4; ++k) a[k] = __builtin_amdgcn_raw_buffer_load_b128(rs, o_bd + k * 1024, 0, AUX_SC1);
        const unsigned mm = max(max(max(mlv[0][0], mlv[0][1]), max(mlv[1][0], mlv[1][1])), max(max(mlv[2][0], mlv[2][1]), max(mlv[3][0], mlv[3][1])));
        ok = max(max(max(umax4(a[0]), umax4(a[1])), max(umax4(a[2]), umax4(a[3]))), mm) != SENT;
      }
      if (__all(ok)) break;
      if (PSLEEP) __builtin_amdgcn_s_sleep(PSLEEP);
    }
    STAMP(6);
#define MF(k, i) __uint_as_float(mlv[k][i])
    const float M = xg_max2(xor8_max(vmax(vmax3(MF(0, 0), MF(1, 0), MF(2, 0)), MF(3, 0))));
    const float w0 = __builtin_amdgcn_exp2f(MF(0, 0) - M), w1_ = __builtin_amdgcn_exp2f(MF(1, 0) - M), w2_ = __builtin_amdgcn_exp2f(MF(2, 0) - M), w3 = __builtin_amdgcn_exp2f(MF(3, 0) - M);
    float L = (w0 * MF(0, 1) + w1_ * MF(1, 1)) + (w2_ * MF(2, 1) + w3 * MF(3, 1));
    const f32x4 v = (__builtin_bit_cast(f32x4, a[0]) * w0 + __builtin_bit_cast(f32x4, a[1]) * w1_) + (__builtin_bit_cast(f32x4, a[2]) * w2_ + __builtin_bit_cast(f32x4, a[3]) * w3);
    L = swap32_add(L, L);
    const float u0 = swap32_add(v[0], v[2]), u1 = swap32_add(v[1], v[3]);
    L = swap16_add(L, L);
    float u = swap16_add(u0, u1);
    L += dpp<XOR8>(L); u += dpp<XOR8>(u);
    const float ov = u * __builtin_amdgcn_rcpf(L);
    *(LAS u16*)(L8 + a_ob) = (u16)pk_bf16(ov, ov);
  }
  BAR();
  {
    f32x4 y = {0.f, 0.f, 0.f, 0.f}, y2 = {0.f, 0.f, 0.f, 0.f};
    const LAS unsigned char* ob = L8 + L_OB + g * 16;
    const u32x4 f0 = *(const LAS u32x4*)ob, f1 = *(const LAS u32x4*)(ob + 64), f2 = *(const LAS u32x4*)(ob + 128), f3 = *(const LAS u32x4*)(ob + 192);
    y = mfma16(wof[0], f0, y); y2 = mfma16(wof[1], f1, y2); y = mfma16(wof[2], f2, y); y2 = mfma16(wof[3], f3, y2);
    u32x4 ub = __builtin_bit_cast(u32x4, y + y2);
#pragma unroll
    for (int k = 0; k < 4; ++k) ub[k] = min(ub[k], SENT - 1u);
    if (c == 0) __builtin_amdgcn_raw_buffer_store_b128(ub, rs, o_y, 0, AUX_SC1);
  }
  STAMP(7);
  if (CD) { const u64 tp = rt(); _Pragma("nounroll") while (rt() - tp < CD) {} }
  {
    f32x4 s4;
    _Pragma("nounroll") for (int sp = 0; sp < SPIN_CAP; ++sp) { CFENCE();
      u32x4 yv[16];
#pragma unroll
      for (int k = 0; k < 16; ++k) yv[k] = __builtin_amdgcn_raw_buffer_load_b128(rs, o_c, k * 4096, AUX_SC1);
#define YF(i) __builtin_bit_cast(f32x4, yv[i])
      s4 = (((YF(0) + YF(1)) + (YF(2) + YF(3))) + ((YF(4) + YF(5)) + (YF(6) + YF(7)))) + (((YF(8) + YF(9)) + (YF(10) + YF(11))) + ((YF(12) + YF(13)) + (YF(14) + YF(15))));
      if (!__any(__builtin_isunordered(s4[0], s4[1]) || __builtin_isunordered(s4[2], s4[3]))) break;
      unsigned mx = 0;
#pragma unroll
      for (int k = 0; k < 16; ++k) mx = max(mx, umax4(yv[k]));
      if (!__any(mx == SENT)) break;
    }
    STAMP(8);
    const f32x2 at = {swap32_add(s4[0], s4[2]), swap32_add(s4[1], s4[3])};
    const f32x2 h = rbf2(xf + rbf2(at));
    const float piv = rdfirst(h[0]);
    const f32x2 d = h - piv, dd = d * d;
    float S, Q;
    wave_sum2(d[0] + d[1], dd[0] + dd[1], S, Q);
    const float md = S * (1.0f / 128.f);
    if (lane == 0) *(LAS f32x2*)(L8 + L_STAT + tw * 8) = f32x2{piv + md, fmaf(-md, S, Q)};
    if (tw == (b >> 6) && lane == ((b >> 1) & 31) + 32 * (b & 1)) *(LAS f32x2*)(L8 + L_STAT + 32) = h;
  }
  BAR();
  const f32x4 sa = *(const LAS f32x4*)(L8 + L_STAT), sb = *(const LAS f32x4*)(L8 + L_STAT + 16);
  const f32x2 hh = *(const LAS f32x2*)(L8 + L_STAT + 32);
  {
    const float mean = 0.25f * ((sa[0] + sa[2]) + (sb[0] + sb[2]));
    const f32x2 e0 = f32x2{sa[0], sa[2]} - mean, e1 = f32x2{sb[0], sb[2]} - mean, ee = e0 * e0 + e1 * e1;
    const float var = fmaxf(fmaf(ee[0] + ee[1], 0.25f, ((sa[1] + sa[3]) + (sb[1] + sb[3])) * (1.0f / D)), 0.f);
    const float mb = rbf(mean), rden = ln_rden(var);
    const float mb2 = rbf((mean - mb) * rden), rden2 = ln_rden(var * rden * rden);
    const f32x2 tt = ln_apply2(ln_apply2(hh, mb, rden), mb2, rden2);
    f32x2 a0 = {0.f, 0.f}, a1 = {0.f, 0.f};
#pragma unroll
    for (int i = 0; i < 7; ++i) { const f32x2 gl = gelu2(tt * w1f[i] + b1f[i]) * w2f[i]; if (i & 1) a1 += gl; else a0 += gl; }
    a0 += a1;
    float s0, s1;
    wave_sum2(a0[0], a0[1], s0, s1);
    if (lane == 0) *(LAS f32x2*)(L8 + L_RED + tw * 8) = f32x2{s0, s1};
  }
  BAR();
  if (wave == HELPER) {
    const f32x4 ra = *(const LAS f32x4*)(L8 + L_RED), rb = *(const LAS f32x4*)(L8 + L_RED + 16);
    const f32x2 y = {(ra[0] + ra[2]) + (rb[0] + rb[2]), (ra[1] + ra[3]) + (rb[1] + rb[3])};
    const f32x2 r = hh + rbf2(rbf2(y) + bbf);
    if (lane == 0) *(GAS unsigned*)(out + 2 * b) = pk_bf16(r[0], r[1]);
  }
  STAMP_OUT();
}

extern "C" void kernel_launch(void* const* d_in, const int* in_sizes, int n_in, void* d_out, int out_size, void* d_ws, size_t ws_size, hipStream_t stream) {
  (void)hipFuncSetAttribute((const void*)k_fused, hipFuncAttributeMaxDynamicSharedMemorySize, L_END);
  hipLaunchKernelGGL(k_fused, dim3(NWG), dim3(512), L_END, stream, (const u16*)d_in[0], (const u16*)d_in[1], (const u16*)d_in[2], (const u16*)d_in[3],
                     (const u16*)d_in[4], (const u16*)d_in[6], (const u16*)d_in[7], (const u16*)d_in[8], (const u16*)d_in[9], (u16*)d_out, (unsigned char*)d_ws);
}
```

```cpp
#define THRESH 5000
#include <hip/hip_runtime.h>
#include <hip/hip_bf16.h>
#include <stdint.h>

#define GAS __attribute__((address_space(1)))
#define LAS __attribute__((address_space(3)))
typedef unsigned u32x4 __attribute__((ext_vector_type(4)));
typedef unsigned u32x2 __attribute__((ext_vector_type(2)));
typedef float f32x4 __attribute__((ext_vector_type(4)));
typedef float f32x2 __attribute__((ext_vector_type(2)));
typedef short bf16x8 __attribute__((ext_vector_type(8)));
typedef short s16x4 __attribute__((ext_vector_type(4)));
typedef __bf16 bf16x2 __attribute__((ext_vector_type(2)));
typedef unsigned short u16;
typedef unsigned long long u64;

#define STAMPS 0
#define INFL 24
#define QD 100
#define RETIRE 8
#define PSLEEP 1
#define QSLEEP 4
#define CD 50
#define BD 0
#define XMAP 0
#define NSTAT 60

constexpr int D = 512, HQ = 32, SK = 65536, DH = 128, FF = 1792;
constexpr int NWG = 256, NSPLIT = 32;
constexpr int NT = 64;
constexpr int NSLOT = 8, SLOT = 16384;
constexpr int NC = 6;
constexpr int HELPER = 5;
constexpr int L_RING = 0;
constexpr int L_WO = NSLOT * SLOT;
constexpr int L_CTRL = L_WO + 16384;
constexpr int C_KL = 0, C_VL = 1, C_NTOT = 2, C_TICKET = 4, C_QCNT = 5, C_QREADY = 6, C_FREE = 16;
constexpr int L_QS = L_CTRL + 256;
constexpr int L_QP = L_QS + 1024;
constexpr int L_MRG = L_QP + 1024;
constexpr int L_MML = L_MRG + NC * 4 * 528;
constexpr int NPOOL = NSPLIT * (NT - NSTAT);
constexpr int L_OB = L_MML + 256;
constexpr int L_STAT = L_OB + 256;
constexpr int L_RED = L_STAT + 64;
constexpr int L_ST = L_RED + 64;
constexpr int L_END = L_ST + (STAMPS ? 512 : 0);
static_assert(L_END <= 160 * 1024, "LDS");
constexpr int PREC = 640;
constexpr int W_EPOCH = 0, W_POOL = 4096, W_Y = 65536, W_QG = 131072, W_PA = 262144, W_END = W_PA + HQ * NSPLIT * PREC;
constexpr int W_STAMP = 1 << 20;
constexpr unsigned SENT = 0xFFFFFFFFu;
constexpr int AUX_SC1 = 16;
constexpr float LOG2E = 1.4426950408889634f;
constexpr int SPIN_CAP = 1 << 18;

__device__ __forceinline__ float bf2f(unsigned b) { return __builtin_bit_cast(float, b << 16); }
__device__ __forceinline__ float bfhi(unsigned b) { return __builtin_bit_cast(float, b & 0xffff0000u); }
__device__ __forceinline__ f32x2 unpk(unsigned u) { return f32x2{bf2f(u), bfhi(u)}; }
__device__ __forceinline__ unsigned pk_bf16(float lo, float hi) {
  f32x2 v = {lo, hi};
  return __builtin_bit_cast(unsigned, __builtin_convertvector(v, bf16x2));
}
__device__ __forceinline__ float rbf(float x) { return bf2f(pk_bf16(x, x)); }
__device__ __forceinline__ f32x2 rbf2(f32x2 v) { return unpk(pk_bf16(v[0], v[1])); }
__device__ __forceinline__ f32x4 mfma16(u32x4 a, u32x4 b, f32x4 c) {
  return __builtin_amdgcn_mfma_f32_16x16x32_bf16(__builtin_bit_cast(bf16x8, a), __builtin_bit_cast(bf16x8, b), c, 0, 0, 0);
}
template <int CTRL> __device__ __forceinline__ float dpp(float x) {
  return __builtin_bit_cast(float, __builtin_amdgcn_mov_dpp(__builtin_bit_cast(int, x), CTRL, 0xf, 0xf, true));
}
constexpr int XOR1 = 0xB1, XOR2 = 0x4E, XOR7 = 0x141, XOR8 = 0x128;
__device__ __forceinline__ float swap16_add(float a, float b) {
  auto s = __builtin_amdgcn_permlane16_swap(__float_as_uint(a), __float_as_uint(b), false, false);
  return __uint_as_float(s[0]) + __uint_as_float(s[1]);
}
__device__ __forceinline__ float swap32_add(float a, float b) {
  auto s = __builtin_amdgcn_permlane32_swap(__float_as_uint(a), __float_as_uint(b), false, false);
  return __uint_as_float(s[0]) + __uint_as_float(s[1]);
}
__device__ __forceinline__ float xg_max(float x) {
  auto s = __builtin_amdgcn_permlane16_swap(__float_as_uint(x), __float_as_uint(x), false, false);
  x = fmaxf(__uint_as_float(s[0]), __uint_as_float(s[1]));
  auto t = __builtin_amdgcn_permlane32_swap(__float_as_uint(x), __float_as_uint(x), false, false);
  return fmaxf(__uint_as_float(t[0]), __uint_as_float(t[1]));
}
__device__ __forceinline__ float vmax(float a, float b) { float r; asm("v_max_f32 %0, %1, %2" : "=v"(r) : "v"(a), "v"(b)); return r; }
__device__ __forceinline__ float vmax3(float a, float b, float c) { float r; asm("v_max3_f32 %0, %1, %2, %3" : "=v"(r) : "v"(a), "v"(b), "v"(c)); return r; }
__device__ __forceinline__ float xg_max2(float x) {
  float t;
  asm("v_mov_b32 %1, %0\n\ts_nop 1\n\tv_permlane16_swap_b32 %0, %1\n\tv_max_f32 %0, %0, %1\n\tv_mov_b32 %1, %0\n\ts_nop 1\n\tv_permlane32_swap_b32 %0, %1\n\tv_max_f32 %0, %0, %1"
      : "+v"(x), "=&v"(t));
  return x;
}
__device__ __forceinline__ float xor8_max(float x) {
  asm("s_nop 1\n\tv_max_f32_dpp %0, %0, %0 row_ror:8 row_mask:0xf bank_mask:0xf" : "+v"(x));
  return x;
}
__device__ __forceinline__ float xg_sum(float x) { x = swap16_add(x, x); return swap32_add(x, x); }
__device__ __forceinline__ float rdlane(float v, int l) { return __builtin_bit_cast(float, __builtin_amdgcn_readlane(__builtin_bit_cast(int, v), l)); }
__device__ __forceinline__ float rdfirst(float v) { return __builtin_bit_cast(float, __builtin_amdgcn_readfirstlane(__builtin_bit_cast(int, v))); }
__device__ __forceinline__ void wave_sum2(float a, float b, float& sa, float& sb) {
  float v = swap32_add(a, b);
  v += dpp<XOR1>(v); v += dpp<XOR2>(v); v += dpp<XOR7>(v); v += dpp<XOR8>(v);
  v = swap16_add(v, v);
  sa = rdlane(v, 0); sb = rdlane(v, 32);
}
__device__ __forceinline__ float ln_rden(float var) { return __builtin_amdgcn_rcpf(rbf(__builtin_amdgcn_sqrtf(rbf(rbf(var) + 1e-5f)))); }
__device__ __forceinline__ f32x2 ln_apply2(f32x2 v, float mb, float rden) { return rbf2(rbf2(v - mb) * rden); }
__device__ __forceinline__ f32x2 gelu2(f32x2 u) {
  constexpr float C0 = 0.7978845608028654f * 2.0f * LOG2E, C1 = C0 * 0.044715f;
  const f32x2 z = u * (u * u * C1 + C0);
  const f32x2 e = {__builtin_amdgcn_exp2f(z[0]), __builtin_amdgcn_exp2f(z[1])};
  const f32x2 e1 = e + 1.0f;
  const f32x2 r = {__builtin_amdgcn_rcpf(e1[0]), __builtin_amdgcn_rcpf(e1[1])};
  return u - u * r;
}
__device__ __forceinline__ unsigned umax4(u32x4 v) { return max(max(v[0], v[1]), max(v[2], v[3])); }
__device__ __forceinline__ u64 rt() { return __builtin_amdgcn_s_memrealtime(); }
#define LDS_WAIT() asm volatile("s_waitcnt lgkmcnt(0)" ::: "memory")
#define CFENCE() asm volatile("" ::: "memory")
#define BAR() do { __builtin_amdgcn_sched_barrier(0); LDS_WAIT(); __builtin_amdgcn_s_barrier(); CFENCE(); __builtin_amdgcn_sched_barrier(0); } while (0)
#define LSPIN(cond) do { int sp_ = 0; _Pragma("nounroll") while (cond) { __builtin_amdgcn_s_sleep(1); if (++sp_ > SPIN_CAP) break; } __builtin_amdgcn_fence(__ATOMIC_ACQUIRE, "workgroup"); } while (0)
#define STAMP(i) do {} while (0)
#define STAMPC(i) do {} while (0)
#define STAMP_OUT() do {} while (0)
#define STR2(x) #x
#define STR(x) STR2(x)

__global__ void __launch_bounds__(512, 2)
k_fused(const u16* __restrict__ x, const u16* __restrict__ K, const u16* __restrict__ V, const u16* __restrict__ wq, const u16* __restrict__ wo,
        const u16* __restrict__ w1, const u16* __restrict__ b1, const u16* __restrict__ w2, const u16* __restrict__ b2, u16* __restrict__ out,
        unsigned char* __restrict__ ws) {
  extern __shared__ __attribute__((aligned(16))) unsigned char lds[];
  LAS unsigned char* L8 = (LAS unsigned char*)lds;
  volatile LAS unsigned* CTRL = (volatile LAS unsigned*)(L8 + L_CTRL);
  const int tid = threadIdx.x, lane = tid & 63, wave = __builtin_amdgcn_readfirstlane(tid >> 6), b = blockIdx.x;
  const int g = lane >> 4, c = lane & 15;
  const unsigned t_begin = (unsigned)rt();
  const int kvh = XMAP ? (b & 7) : (b >> 5), split = XMAP ? (b >> 3) : (b & 31);
  const int bhead = 4 * (b & 7) + (b >> 6), bcg = (b >> 3) & 7;
  const __amdgpu_buffer_rsrc_t rs = __builtin_amdgcn_make_buffer_rsrc((void*)ws, 0, W_END, 0x00020000);
  {
    const u64 kb = (u64)K + (u64)kvh * (SK * DH * 2) - 3072, vb = (u64)V + (u64)kvh * (SK * DH * 2), pool = (u64)ws + W_POOL + kvh * 256;
    const unsigned vk0 = (4 * (c & 3) + (c >> 2)) * 256 + g * 16 + 3072;
    const unsigned vinit = lane == C_NTOT ? 0x7fffffffu : 0u;
    unsigned sq, st, st1, st2, spk, spv, sa, sb, sm0;
    unsigned vt, va, vc;
#define GL(v, off) "global_load_lds_dwordx4 %[" v "], s[90:91] offset:" off " nt\n\t"
#define PUB(reg, off) "v_mov_b32 %[vt], %[" reg "]\n\tds_write_b32 %[vc], %[vt] offset:" off "\n\t"
#define LST(i)
#define LSTQ(qq, i)
    asm volatile(
        "s_cmp_lg_u32 %[wave], 0\n\t"
        "s_cbranch_scc1 Lld_end\n\t"
        "s_mov_b32 %[sq], 0\n\t"
        "s_mov_b32 %[spk], 0\n\t"
        "s_mov_b32 %[spv], 0\n\t"
        "v_mov_b32 %[vc], %[ctrl]\n\t"
        "v_lshrrev_b32 %[vt], 2, %[vv]\n\t"
        "v_add_u32 %[vt], %[vc], %[vt]\n\t"
        "ds_write_b32 %[vt], %[vinit]\n\t"
        LST("12")
        "Lld_loop:\n\t"
        "s_cmp_lt_u32 %[sq], %[nstat]-2\n\t"
        "s_cbranch_scc1 Lld_noclaim\n\t"
        "s_add_u32 %[sa], %[sq], 2\n\t"
        "s_lshl_b64 exec, 1, %[sa]\n\t"
        "v_mov_b32 %[vt], 0\n\t"
        "v_mov_b32 %[va], 1\n\t"
        "global_atomic_add %[va], %[vt], %[va], %[pool] sc0\n\t"
        "s_mov_b64 exec, -1\n\t"
        "Lld_noclaim:\n\t"
        "s_add_u32 %[st], %[tile0], %[sq]\n\t"
        "s_cmp_lt_u32 %[sq], %[nstat]\n\t"
        "s_cbranch_scc1 Lld_havetile\n\t"
        "v_readlane_b32 %[sa], %[va], %[sq]\n\t"
        "s_nop 0\n\t"
        "s_cmp_ge_u32 %[sa], %[npool]\n\t"
        "s_cbranch_scc1 Lld_tail\n\t"
        "s_and_b32 %[st], %[sa], 31\n\t"
        "s_lshl_b32 %[st], %[st], 6\n\t"
        "s_lshr_b32 %[sa], %[sa], 5\n\t"
        "s_add_u32 %[st], %[st], %[sa]\n\t"
        "s_add_u32 %[st], %[st], %[nstat]\n\t"
        "Lld_havetile:\n\t"
        "s_cmp_lt_u32 %[sq], 8\n\t"
        "s_cbranch_scc1 Lld_free\n\t"
        "s_and_b32 %[sa], %[sq], 7\n\t"
        "s_lshl_b32 %[sa], %[sa], 2\n\t"
        "s_mov_b32 %[sb], %[cap]\n\t"
        "Lld_spin:\n\t"
        "v_add_u32 %[vt], %[sa], %[vc]\n\t"
        "ds_read_b32 %[vt], %[vt] offset:64\n\t"
        "s_waitcnt lgkmcnt(0)\n\t"
        "v_readfirstlane_b32 %[sm0], %[vt]\n\t"
        "s_nop 0\n\t"
        "s_add_u32 %[sm0], %[sm0], 7\n\t"
        "s_cmp_ge_u32 %[sm0], %[sq]\n\t"
        "s_cbranch_scc1 Lld_free\n\t"
        "s_waitcnt vmcnt(0)\n\t"
        "s_mov_b32 %[spk], %[sq]\n\t"
        "s_sub_u32 %[spv], %[sq], 2\n\t"
        PUB("spk", "0") PUB("spv", "4")
        "s_sleep 1\n\t"
        "s_sub_u32 %[sb], %[sb], 1\n\t"
        "s_cmp_lg_u32 %[sb], 0\n\t"
        "s_cbranch_scc1 Lld_spin\n\t"
        "Lld_free:\n\t"
        "s_lshl_b32 %[sa], %[st], 13\n\t"
        "s_add_u32 s90, %[kbl], %[sa]\n\t"
        "s_addc_u32 s91, %[kbh], 0\n\t"
        "s_and_b32 %[sa], %[sq], 7\n\t"
        "s_lshl_b32 m0, %[sa], 14\n\t"
        "s_nop 0\n\t"
        GL("vk0", "0") GL("vk1", "1024") GL("vk2", "2048") GL("vk3", "3072")
        "s_addk_i32 m0, 0x1000\n\t"
        "s_add_u32 s90, s90, 0x1000\n\t"
        "s_addc_u32 s91, s91, 0\n\t"
        GL("vk0", "0") GL("vk1", "1024") GL("vk2", "2048") GL("vk3", "3072")
        "s_cmp_lt_u32 %[sq], 2\n\t"
        "s_cbranch_scc1 Lld_next\n\t"
        "s_waitcnt vmcnt(24)\n\t"
        "s_sub_i32 %[sa], %[sq], 3\n\t"
        "s_max_i32 %[spv], %[spv], %[sa]\n\t"
        PUB("spv", "4")
        "s_lshl_b32 %[sa], %[st2], 13\n\t"
        "s_add_u32 s90, %[vbl], %[sa]\n\t"
        "s_addc_u32 s91, %[vbh], 0\n\t"
        "s_sub_u32 %[sa], %[sq], 2\n\t"
        "s_and_b32 %[sa], %[sa], 7\n\t"
        "s_lshl_b32 %[sa], %[sa], 14\n\t"
        "s_add_u32 m0, %[sa], 0x2000\n\t"
        "s_nop 0\n\t"
        GL("vv", "0") GL("vv", "1024") GL("vv", "2048") GL("vv", "3072")
        "s_addk_i32 m0, 0x1000\n\t"
        "s_add_u32 s90, s90, 0x1000\n\t"
        "s_addc_u32 s91, s91, 0\n\t"
        GL("vv", "0") GL("vv", "1024") GL("vv", "2048") GL("vv", "3072")
        "s_cmp_lg_u32 %[sq], 2\n\t"
        "s_cbranch_scc1 Lld_nobar\n\t"
        LST("13")
        "s_waitcnt lgkmcnt(0)\n\t"
        "s_barrier\n\t"
        "s_branch Lld_next\n\t"
        "Lld_nobar:\n\t"
        "s_waitcnt vmcnt(24)\n\t"
        "s_max_i32 %[spk], %[spk], %[sq]\n\t"
        PUB("spk", "0")
        LSTQ("4", "2") LSTQ("8", "3") LSTQ("16", "4") LSTQ("32", "10") LSTQ("48", "11")
        "Lld_next:\n\t"
        "s_mov_b32 %[st2], %[st1]\n\t"
        "s_mov_b32 %[st1], %[st]\n\t"
        "s_add_u32 %[sq], %[sq], 1\n\t"
        "s_branch Lld_loop\n\t"
        "Lld_tail:\n\t"
        PUB("sq", "8")
        "s_mov_b32 %[sb], 2\n\t"
        "Lld_tail2:\n\t"
        "s_lshl_b32 %[sa], %[st2], 13\n\t"
        "s_add_u32 s90, %[vbl], %[sa]\n\t"
        "s_addc_u32 s91, %[vbh], 0\n\t"
        "s_sub_u32 %[sa], %[sq], %[sb]\n\t"
        "s_and_b32 %[sa], %[sa], 7\n\t"
        "s_lshl_b32 %[sa], %[sa], 14\n\t"
        "s_add_u32 m0, %[sa], 0x2000\n\t"
        "s_nop 0\n\t"
        GL("vv", "0") GL("vv", "1024") GL("vv", "2048") GL("vv", "3072")
        "s_addk_i32 m0, 0x1000\n\t"
        "s_add_u32 s90, s90, 0x1000\n\t"
        "s_addc_u32 s91, s91, 0\n\t"
        GL("vv", "0") GL("vv", "1024") GL("vv", "2048") GL("vv", "3072")
        "s_mov_b32 %[st2], %[st1]\n\t"
        "s_sub_u32 %[sb], %[sb], 1\n\t"
        "s_cmp_lg_u32 %[sb], 0\n\t"
        "s_cbranch_scc1 Lld_tail2\n\t"
        "s_waitcnt vmcnt(32)\n\t"
        "s_sub_i32 %[sa], %[sq], 3\n\t"
        "s_max_i32 %[spv], %[spv], %[sa]\n\t"
        PUB("spv", "4")
        "s_waitcnt vmcnt(24)\n\t"
        PUB("sq", "0")
        LST("5")
        "s_sub_u32 %[spv], %[sq], 2\n\t"
        "s_waitcnt vmcnt(16)\n\t"
        PUB("spv", "4")
        "s_add_u32 %[spv], %[spv], 1\n\t"
        "s_waitcnt vmcnt(8)\n\t"
        PUB("spv", "4")
        "s_waitcnt vmcnt(0)\n\t"
        PUB("sq", "4")
        LST("1")
        "s_waitcnt lgkmcnt(0)\n\t"
        "s_endpgm\n\t"
        "Lld_end:"
        : [sq] "=&s"(sq), [st] "=&s"(st), [st1] "=&s"(st1), [st2] "=&s"(st2), [spk] "=&s"(spk), [spv] "=&s"(spv), [sa] "=&s"(sa), [sb] "=&s"(sb), [sm0] "=&s"(sm0),
          [vt] "=&v"(vt), [va] "=&v"(va), [vc] "=&v"(vc)
        : [wave] "s"(wave), [kbl] "s"((unsigned)kb), [kbh] "s"((unsigned)(kb >> 32)), [vbl] "s"((unsigned)vb), [vbh] "s"((unsigned)(vb >> 32)), [pool] "s"(pool), [tile0] "s"(split * NT), [vk0] "v"(vk0), [vk1] "v"(vk0 + 64 - 1024), [vk2] "v"(vk0 + 128 - 2048),
          [vk3] "v"(vk0 + 192 - 3072), [vv] "v"(lane * 16), [vinit] "v"(vinit), [ctrl] "i"(L_CTRL), [nstat] "i"(NSTAT), [npool] "i"(NPOOL), [cap] "i"(SPIN_CAP), [st0] "i"(L_ST - L_CTRL),
          "s"(x), "s"(wq), "s"(wo), "s"(w1), "s"(b1), "s"(w2), "s"(b2), "s"(out)
        : "memory", "scc", "s90", "s91", "s92", "s93");
  }
  if (wave <= 4) {
    const u32x4 xv = *(const GAS u32x4*)(x + 8 * lane);
    const int row0 = (split & 3) * 128 + 32 * (wave - 1) + 4 * (lane >> 3);
    const u32x2 xo = *(const GAS u32x2*)(x + row0);
    u32x4 wqv[4];
    const u16* wp = wq + (size_t)row0 * (HQ * DH) + kvh * 512 + (split >> 2) * 64 + 8 * (lane & 7);
#pragma unroll
    for (int i = 0; i < 4; ++i) wqv[i] = __builtin_nontemporal_load((const GAS u32x4*)(wp + i * (HQ * DH)));
    BAR();
    const float piv = rdfirst(bf2f(xv[0]));
    const f32x2 d0 = unpk(xv[0]) - piv, d1 = unpk(xv[1]) - piv, d2 = unpk(xv[2]) - piv, d3 = unpk(xv[3]) - piv;
    const f32x2 s = (d0 + d1) + (d2 + d3), qq = (d0 * d0 + d1 * d1) + (d2 * d2 + d3 * d3);
    float S, Q;
    wave_sum2(s[0] + s[1], qq[0] + qq[1], S, Q);
    const float md = S * (1.0f / D);
    const float mb = rbf(piv + md), rden = ln_rden(fmaxf(fmaf(-md, md, Q * (1.0f / D)), 0.f));
    const f32x2 xa = ln_apply2(unpk(xo[0]), mb, rden), xb = ln_apply2(unpk(xo[1]), mb, rden);
    STAMP(10);
    f32x2 a2[4];
#pragma unroll
    for (int j = 0; j < 4; ++j) a2[j] = (unpk(wqv[0][j]) * xa[0] + unpk(wqv[1][j]) * xa[1]) + (unpk(wqv[2][j]) * xb[0] + unpk(wqv[3][j]) * xb[1]);
    float a[8];
#pragma unroll
    for (int j = 0; j < 8; ++j) a[j] = a2[j >> 1][j & 1];
#pragma unroll
    for (int j = 0; j < 4; ++j) a[j] = swap32_add(a[j], a[j + 4]);
#pragma unroll
    for (int j = 0; j < 2; ++j) a[j] = swap16_add(a[j], a[j + 2]);
    { const float keep = (lane & 8) ? a[1] : a[0], send = (lane & 8) ? a[0] : a[1]; a[0] = keep + dpp<XOR8>(send); }
    ((LAS float*)(L8 + L_QP))[(wave - 1) * 64 + 8 * (lane & 7) + (lane >> 3)] = a[0];
    LDS_WAIT();
    if (lane == 0) __hip_atomic_fetch_add((LAS unsigned*)(L8 + L_CTRL) + C_QCNT, 1u, __ATOMIC_RELAXED, __HIP_MEMORY_SCOPE_WORKGROUP);
    STAMP(1);
  } else if (wave == HELPER) {
    const u64 epoch = __hip_atomic_load((GAS u64*)(ws + W_EPOCH), __ATOMIC_RELAXED, __HIP_MEMORY_SCOPE_AGENT);
    const u32x4 sv = {SENT, SENT, SENT, SENT};
    if (lane < 40) {
#pragma unroll
      for (int h = 0; h < 4; ++h) __builtin_amdgcn_raw_buffer_store_b128(sv, rs, W_PA + (((4 * kvh + h) * 5 + (lane >> 3)) * NSPLIT + split) * 128 + (lane & 7) * 16, 0, AUX_SC1);
    }
    if (lane < 16) __builtin_amdgcn_raw_buffer_store_b128(sv, rs, W_Y + (bhead * D + bcg * 64) * 4 + lane * 16, 0, AUX_SC1);
    if (split == 0 && lane == 0) __builtin_amdgcn_raw_buffer_store_b32(0u, rs, W_POOL + kvh * 256, 0, AUX_SC1);
    BAR();
    asm volatile("s_waitcnt vmcnt(0)" ::: "memory");
    STAMP(10);
    const u64 tag = (epoch + 1) << 24;
    const unsigned tlo = (unsigned)tag, thi = (unsigned)(tag >> 32);
    LSPIN(CTRL[C_QCNT] < 4u);
    const LAS float* qp = (const LAS float*)(L8 + L_QP);
    const unsigned qs = __float_as_uint((qp[lane] + qp[64 + lane]) + (qp[128 + lane] + qp[192 + lane]));
    __hip_atomic_store((GAS u64*)(ws + W_QG) + (kvh * 4 + (split & 3)) * 512 + (split >> 2) * 64 + lane, tag | (u64)((qs + 0x80u) >> 8), __ATOMIC_RELAXED, __HIP_MEMORY_SCOPE_AGENT);
    STAMP(1);
    { const u64 tp = rt(); _Pragma("nounroll") while (rt() - tp < QD) __builtin_amdgcn_s_sleep(2); }
    STAMP(12);
    {
      u32x4 gq[16];
      _Pragma("nounroll") for (int sp = 0; sp < SPIN_CAP; ++sp) { CFENCE();
#pragma unroll
        for (int k = 0; k < 16; ++k) gq[k] = __builtin_amdgcn_raw_buffer_load_b128(rs, W_QG + kvh * 16384 + k * 1024 + lane * 16, 0, AUX_SC1);
        unsigned bad = 0;
#pragma unroll
        for (int k = 0; k < 16; ++k) bad |= (((gq[k][0] ^ tlo) | (gq[k][2] ^ tlo)) & 0xff000000u) | (gq[k][1] ^ thi) | (gq[k][3] ^ thi);
        if (__all(bad == 0u)) break;
        __builtin_amdgcn_s_sleep(QSLEEP);
      }
#pragma unroll
      for (int k = 0; k < 4; ++k) {
        const float lo = (__uint_as_float(gq[k][0] << 8) + __uint_as_float(gq[4 + k][0] << 8)) + (__uint_as_float(gq[8 + k][0] << 8) + __uint_as_float(gq[12 + k][0] << 8));
        const float hi = (__uint_as_float(gq[k][2] << 8) + __uint_as_float(gq[4 + k][2] << 8)) + (__uint_as_float(gq[8 + k][2] << 8) + __uint_as_float(gq[12 + k][2] << 8));
        *(LAS unsigned*)(L8 + L_QS + k * 256 + lane * 4) = pk_bf16(lo, hi);
      }
      LDS_WAIT();
      CTRL[C_QREADY] = 1u;
    }
    STAMP(2);
    {
      const u16* wop = wo + (size_t)(bhead * DH + (lane >> 3)) * D + bcg * 64 + 8 * (lane & 7);
#pragma unroll
      for (int i = 0; i < 16; ++i) __builtin_amdgcn_global_load_lds((const unsigned*)(wop + i * 8 * D), (unsigned*)(lds + L_WO + i * 1024), 16, 0, 2);
    }
    LSPIN(CTRL[C_KL] < (unsigned)(NT / 2));
    _Pragma("nounroll") for (int sp = 0; sp < SPIN_CAP; ++sp) { CFENCE();
      unsigned bad = 0;
#pragma unroll
      for (int k = 0; k < 4; ++k) {
        const u32x2 t = __builtin_amdgcn_raw_buffer_load_b64(rs, W_QG + (lane + 64 * k) * 512, 0, AUX_SC1);
        bad |= ((t[0] ^ tlo) & 0xff000000u) | (t[1] ^ thi);
      }
      if (__all(bad == 0u)) break;
      __builtin_amdgcn_s_sleep(8);
    }
    if (b == 0 && lane == 0) __hip_atomic_store((GAS u64*)(ws + W_EPOCH), epoch + 1, __ATOMIC_RELAXED, __HIP_MEMORY_SCOPE_AGENT);
    asm volatile("s_waitcnt vmcnt(0)" ::: "memory");
    STAMP(11);
  } else {
    BAR();
  }

  const int tw = wave & 3;
  unsigned pw[7], pz[7], xr = 0, pbb = 0;
  if (wave >= 4) {
    const int t = tid - 256;
#pragma unroll
    for (int i = 0; i < 7; ++i) { pw[i] = (unsigned)w1[t + 256 * i] | ((unsigned)b1[t + 256 * i] << 16); pz[i] = w2[t + 256 * i]; }
    xr = *(const GAS unsigned*)(x + 128 * tw + 4 * (lane & 31) + 2 * (lane >> 5));
    pbb = *(const GAS u16*)b2;
  } else {
#pragma unroll
    for (int i = 0; i < 7; ++i) { pw[i] = 0; pz[i] = 0; }
  }
  if (wave != HELPER) {
    f32x4 o[8];
#pragma unroll
    for (int d = 0; d < 8; ++d) o[d] = f32x4{0.f, 0.f, 0.f, 0.f};
    float m = -INFINITY, l = 0.f;
    u32x4 qf[4];
#pragma unroll
    for (int s = 0; s < 4; ++s) qf[s] = u32x4{0u, 0u, 0u, 0u};
    constexpr float SCALE = 0.08838834764831845f;
#define TICKET() __builtin_amdgcn_readfirstlane(lane == 0 ? __hip_atomic_fetch_add((LAS unsigned*)(L8 + L_CTRL) + C_TICKET, 1u, __ATOMIC_RELAXED, __HIP_MEMORY_SCOPE_WORKGROUP) : 0u)
    const LAS unsigned char* ring = L8 + L_RING + lane * 16;
    unsigned q = TICKET();
    for (int it = 0;; ++it) {
      u32x4 tk[8], tv[8];
      const int s = q & (NSLOT - 1);
      const LAS unsigned char* sp = ring + s * SLOT;
      unsigned ntot;
#define WAIT_LANDED(idx) do { int sp_ = 0; _Pragma("nounroll") for (;;) { const u32x4 cw_ = *(const volatile LAS u32x4*)(L8 + L_CTRL); \
        ntot = __builtin_amdgcn_readfirstlane(cw_[C_NTOT]); \
        if (__builtin_amdgcn_readfirstlane(cw_[idx]) > q || ntot <= q || ++sp_ > SPIN_CAP) break; __builtin_amdgcn_s_sleep(1); } \
        __builtin_amdgcn_fence(__ATOMIC_ACQUIRE, "workgroup"); } while (0)
#define READ_V() do { WAIT_LANDED(C_VL); \
      _Pragma("unroll") for (int j = 0; j < 8; ++j) tv[j] = *(const LAS u32x4*)(sp + 8192 + j * 1024); \
      LDS_WAIT(); CTRL[C_FREE + s] = q + 1; \
      asm volatile("" : "+v"(tv[0]), "+v"(tv[1]), "+v"(tv[2]), "+v"(tv[3]), "+v"(tv[4]), "+v"(tv[5]), "+v"(tv[6]), "+v"(tv[7]) :: "memory"); } while (0)
      WAIT_LANDED(C_KL);
      if (ntot <= q || q >= (unsigned)(NT + NPOOL)) break;
      if (wave < 4) STAMP(5);
#pragma unroll
      for (int j = 0; j < 8; ++j) tk[j] = *(const LAS u32x4*)(sp + j * 1024);
      if (it == 0) {
        READ_V();
        LSPIN(CTRL[C_QREADY] == 0u);
        if (c < 4) {
#pragma unroll
          for (int s2 = 0; s2 < 4; ++s2) qf[s2] = *(const LAS u32x4*)(L8 + L_QS + c * 256 + s2 * 64 + g * 16);
        }
        STAMP(3);
      }
      f32x4 s0 = {0.f, 0.f, 0.f, 0.f}, s1 = {0.f, 0.f, 0.f, 0.f};
#pragma unroll
      for (int k = 0; k < 4; ++k) { s0 = mfma16(tk[k], qf[k], s0); s1 = mfma16(tk[4 + k], qf[k], s1); }
      unsigned qn = 0x7fffffffu;
      if (wave < 4 || q < (unsigned)(NSTAT - RETIRE)) qn = TICKET();
      f32x2 sc[4];
      sc[0] = rbf2(rbf2(f32x2{s0[0], s0[1]}) * SCALE); sc[1] = rbf2(rbf2(f32x2{s0[2], s0[3]}) * SCALE);
      sc[2] = rbf2(rbf2(f32x2{s1[0], s1[1]}) * SCALE); sc[3] = rbf2(rbf2(f32x2{s1[2], s1[3]}) * SCALE);
      const float mx = xg_max2(vmax3(vmax3(sc[0][0], sc[0][1], sc[1][0]), vmax3(sc[1][1], sc[2][0], sc[2][1]), vmax(sc[3][0], sc[3][1])));
      if (__any(mx > m)) {
        const float mn = vmax(m, mx);
        const float alpha = __builtin_amdgcn_exp2f((m - mn) * LOG2E);
        m = mn; l *= alpha;
#pragma unroll
        for (int d = 0; d < 8; ++d) o[d] *= alpha;
      }
      const float ms = m * LOG2E;
      float p[8];
#pragma unroll
      for (int r = 0; r < 8; ++r) p[r] = __builtin_amdgcn_exp2f(fmaf(sc[r >> 1][r & 1], LOG2E, -ms));
      l += xg_sum(((p[0] + p[1]) + (p[2] + p[3])) + ((p[4] + p[5]) + (p[6] + p[7])));
      u32x4 pf = {pk_bf16(p[0], p[1]), pk_bf16(p[2], p[3]), pk_bf16(p[4], p[5]), pk_bf16(p[6], p[7])};
      asm volatile("" : "+v"(pf), "+v"(l));
      if (wave < 4) STAMP(6);
      if (it != 0) READ_V();
      if (wave < 4) STAMP(7);
#pragma unroll
      for (int d = 0; d < 8; ++d) {
        u32x4 a;
#pragma unroll
        for (int jj = 0; jj < 4; ++jj) a[jj] = __builtin_amdgcn_perm(tv[2 * jj + 1][d >> 1], tv[2 * jj][d >> 1], (d & 1) ? 0x07060302u : 0x05040100u);
        o[d] = mfma16(a, pf, o[d]);
      }
      if (qn >= ntot) break;
      q = qn;
    }
    if (c < 4) {
      const int ci = wave > HELPER ? wave - 2 : wave - 1;
      LAS float* rec = (LAS float*)(L8 + L_MRG) + (ci * 4 + c) * 132;
      if (g == 0) *(LAS f32x2*)(L8 + L_MML + c * 64 + ci * 8) = f32x2{m * LOG2E, l};
#pragma unroll
      for (int d = 0; d < 8; ++d) *(LAS f32x4*)(rec + d * 16 + g * 4) = o[d];
    }
    STAMP(4);
  }
  if (wave < 4) {
    BAR(); STAMP_OUT();
    return;
  }

  float w1f[7], b1f[7], w2f[7];
#pragma unroll
  for (int i = 0; i < 7; ++i) { w1f[i] = bf2f(pw[i]); b1f[i] = bfhi(pw[i]); w2f[i] = bf2f(pz[i]); asm volatile("" : "+v"(w1f[i]), "+v"(b1f[i]), "+v"(w2f[i])); }
  const int hb = lane >> 5;
  int a_rec = L_MRG + (hb * 12 + tw) * 528 + (lane & 31) * 16;
  int a_mml = L_MML + tw * 64 + hb * 24;
  int o_pa = W_PA + (((4 * kvh + tw) * 5 + (lane >> 3)) * NSPLIT + split) * 128 + (lane & 7) * 16;
  int o_bd = W_PA + (bhead * 5 + tw) * 4096 + lane * 16, o_bm = W_PA + (bhead * 5 + 4) * 4096 + lane * 16;
  int o_y = W_Y + (bhead * D + bcg * 64 + 16 * tw + 4 * g) * 4;
  int o_c = W_Y + (hb * D + 128 * tw + 4 * (lane & 31)) * 4;
  int a_ob = L_OB + (tw * 32 + 4 * (lane & 7) + (lane >> 4)) * 2;
  f32x2 xf = unpk(xr); float bbf = bf2f(pbb);
  asm volatile("" : "+v"(a_rec), "+v"(a_mml), "+v"(o_pa), "+v"(o_bd), "+v"(o_bm), "+v"(o_y), "+v"(o_c), "+v"(a_ob), "+v"(xf), "+v"(bbf));
  BAR();
  {
    const f32x2 m0 = *(const LAS f32x2*)(L8 + a_mml), m1 = *(const LAS f32x2*)(L8 + a_mml + 8), m2 = *(const LAS f32x2*)(L8 + a_mml + 16);
    const f32x4 r0 = *(const LAS f32x4*)(L8 + a_rec), r1 = *(const LAS f32x4*)(L8 + a_rec + 2112), r2 = *(const LAS f32x4*)(L8 + a_rec + 4224);
    float M = vmax3(m0[0], m1[0], m2[0]), t;
    asm("v_mov_b32 %1, %0\n\ts_nop 1\n\tv_permlane32_swap_b32 %0, %1\n\tv_max_f32 %0, %0, %1" : "+v"(M), "=&v"(t));
    const float e0 = __builtin_amdgcn_exp2f(m0[0] - M), e1 = __builtin_amdgcn_exp2f(m1[0] - M), e2 = __builtin_amdgcn_exp2f(m2[0] - M);
    const float Lp = e0 * m0[1] + e1 * m1[1] + e2 * m2[1];
    const f32x4 acc = r0 * e0 + r1 * e1 + r2 * e2;
    u32x4 ub;
    ub[0] = __float_as_uint(swap32_add(acc[0], 0.5f * M));
    ub[1] = __float_as_uint(swap32_add(acc[1], Lp));
    ub[2] = __float_as_uint(swap32_add(acc[2], Lp));
    ub[3] = __float_as_uint(swap32_add(acc[3], Lp));
#pragma unroll
    for (int k = 0; k < 4; ++k) ub[k] = min(ub[k], SENT - 1u);
    if (lane < 40) __builtin_amdgcn_raw_buffer_store_b128(ub, rs, o_pa, 0, AUX_SC1);
  }
  STAMP(5);
  u32x4 wof[4];
#pragma unroll
  for (int s = 0; s < 4; ++s) {
    const int a = L_WO + (64 * (g & 1) + 8 * (c >> 2) + 2 * s + (g >> 1)) * 128 + tw * 32 + (c & 3) * 8;
    const u32x2 lo = __builtin_bit_cast(u32x2, __builtin_amdgcn_ds_read_tr16_b64_v4i16((LAS s16x4*)(L8 + a)));
    const u32x2 hi = __builtin_bit_cast(u32x2, __builtin_amdgcn_ds_read_tr16_b64_v4i16((LAS s16x4*)(L8 + a + 32 * 128)));
    wof[s] = u32x4{lo[0], lo[1], hi[0], hi[1]};
  }
  if (BD) { const u64 tp = rt(); _Pragma("nounroll") while (rt() - tp < BD) {} }
  {
    u32x4 a[4]; u32x2 mlv[4];
#pragma unroll
    for (int k = 0; k < 4; ++k) { a[k] = u32x4{0u, 0u, 0u, 0u}; mlv[k] = u32x2{0u, 0u}; }
    bool ok = false;
    _Pragma("nounroll") for (int sp = 0; sp < SPIN_CAP; ++sp) { CFENCE();
      if (!ok) {
#pragma unroll
        for (int k = 0; k < 4; ++k) mlv[k] = __builtin_amdgcn_raw_buffer_load_b64(rs, o_bm + k * 1024, 0, AUX_SC1);
#pragma unroll
        for (int k = 0; k < 4; ++k) a[k] = __builtin_amdgcn_raw_buffer_load_b128(rs, o_bd + k * 1024, 0, AUX_SC1);
        const unsigned mm = max(max(max(mlv[0][0], mlv[0][1]), max(mlv[1][0], mlv[1][1])), max(max(mlv[2][0], mlv[2][1]), max(mlv[3][0], mlv[3][1])));
        ok = max(max(max(umax4(a[0]), umax4(a[1])), max(umax4(a[2]), umax4(a[3]))), mm) != SENT;
      }
      if (__all(ok)) break;
      if (PSLEEP) __builtin_amdgcn_s_sleep(PSLEEP);
    }
    STAMP(6);
#define MF(k, i) __uint_as_float(mlv[k][i])
    const float M = xg_max2(xor8_max(vmax(vmax3(MF(0, 0), MF(1, 0), MF(2, 0)), MF(3, 0))));
    const float w0 = __builtin_amdgcn_exp2f(MF(0, 0) - M), w1_ = __builtin_amdgcn_exp2f(MF(1, 0) - M), w2_ = __builtin_amdgcn_exp2f(MF(2, 0) - M), w3 = __builtin_amdgcn_exp2f(MF(3, 0) - M);
    float L = (w0 * MF(0, 1) + w1_ * MF(1, 1)) + (w2_ * MF(2, 1) + w3 * MF(3, 1));
    const f32x4 v = (__builtin_bit_cast(f32x4, a[0]) * w0 + __builtin_bit_cast(f32x4, a[1]) * w1_) + (__builtin_bit_cast(f32x4, a[2]) * w2_ + __builtin_bit_cast(f32x4, a[3]) * w3);
    L = swap32_add(L, L);
    const float u0 = swap32_add(v[0], v[2]), u1 = swap32_add(v[1], v[3]);
    L = swap16_add(L, L);
    float u = swap16_add(u0, u1);
    L += dpp<XOR8>(L); u += dpp<XOR8>(u);
    const float ov = u * __builtin_amdgcn_rcpf(L);
    *(LAS u16*)(L8 + a_ob) = (u16)pk_bf16(ov, ov);
  }
  BAR();
  {
    f32x4 y = {0.f, 0.f, 0.f, 0.f}, y2 = {0.f, 0.f, 0.f, 0.f};
    const LAS unsigned char* ob = L8 + L_OB + g * 16;
    const u32x4 f0 = *(const LAS u32x4*)ob, f1 = *(const LAS u32x4*)(ob + 64), f2 = *(const LAS u32x4*)(ob + 128), f3 = *(const LAS u32x4*)(ob + 192);
    y = mfma16(wof[0], f0, y); y2 = mfma16(wof[1], f1, y2); y = mfma16(wof[2], f2, y); y2 = mfma16(wof[3], f3, y2);
    u32x4 ub = __builtin_bit_cast(u32x4, y + y2);
#pragma unroll
    for (int k = 0; k < 4; ++k) ub[k] = min(ub[k], SENT - 1u);
    if (c == 0) __builtin_amdgcn_raw_buffer_store_b128(ub, rs, o_y, 0, AUX_SC1);
  }
  STAMP(7);
  if (CD) { const u64 tp = rt(); _Pragma("nounroll") while (rt() - tp < CD) {} }
  {
    f32x4 s4;
    _Pragma("nounroll") for (int sp = 0; sp < SPIN_CAP; ++sp) { CFENCE();
      u32x4 yv[16];
#pragma unroll
      for (int k = 0; k < 16; ++k) yv[k] = __builtin_amdgcn_raw_buffer_load_b128(rs, o_c, k * 4096, AUX_SC1);
#define YF(i) __builtin_bit_cast(f32x4, yv[i])
      s4 = (((YF(0) + YF(1)) + (YF(2) + YF(3))) + ((YF(4) + YF(5)) + (YF(6) + YF(7)))) + (((YF(8) + YF(9)) + (YF(10) + YF(11))) + ((YF(12) + YF(13)) + (YF(14) + YF(15))));
      if (!__any(__builtin_isunordered(s4[0], s4[1]) || __builtin_isunordered(s4[2], s4[3]))) break;
      unsigned mx = 0;
#pragma unroll
      for (int k = 0; k < 16; ++k) mx = max(mx, umax4(yv[k]));
      if (!__any(mx == SENT)) break;
    }
    STAMP(8);
    const f32x2 at = {swap32_add(s4[0], s4[2]), swap32_add(s4[1], s4[3])};
    const f32x2 h = rbf2(xf + rbf2(at));
    const float piv = rdfirst(h[0]);
    const f32x2 d = h - piv, dd = d * d;
    float S, Q;
    wave_sum2(d[0] + d[1], dd[0] + dd[1], S, Q);
    const float md = S * (1.0f / 128.f);
    if (lane == 0) *(LAS f32x2*)(L8 + L_STAT + tw * 8) = f32x2{piv + md, fmaf(-md, S, Q)};
    if (tw == (b >> 6) && lane == ((b >> 1) & 31) + 32 * (b & 1)) *(LAS f32x2*)(L8 + L_STAT + 32) = h;
  }
  BAR();
  const f32x4 sa = *(const LAS f32x4*)(L8 + L_STAT), sb = *(const LAS f32x4*)(L8 + L_STAT + 16);
  const f32x2 hh = *(const LAS f32x2*)(L8 + L_STAT + 32);
  {
    const float mean = 0.25f * ((sa[0] + sa[2]) + (sb[0] + sb[2]));
    const f32x2 e0 = f32x2{sa[0], sa[2]} - mean, e1 = f32x2{sb[0], sb[2]} - mean, ee = e0 * e0 + e1 * e1;
    const float var = fmaxf(fmaf(ee[0] + ee[1], 0.25f, ((sa[1] + sa[3]) + (sb[1] + sb[3])) * (1.0f / D)), 0.f);
    const float mb = rbf(mean), rden = ln_rden(var);
    const float mb2 = rbf((mean - mb) * rden), rden2 = ln_rden(var * rden * rden);
    const f32x2 tt = ln_apply2(ln_apply2(hh, mb, rden), mb2, rden2);
    f32x2 a0 = {0.f, 0.f}, a1 = {0.f, 0.f};
#pragma unroll
    for (int i = 0; i < 7; ++i) { const f32x2 gl = gelu2(tt * w1f[i] + b1f[i]) * w2f[i]; if (i & 1) a1 += gl; else a0 += gl; }
    a0 += a1;
    float s0, s1;
    wave_sum2(a0[0], a0[1], s0, s1);
    if (lane == 0) *(LAS f32x2*)(L8 + L_RED + tw * 8) = f32x2{s0, s1};
  }
  BAR();
  if (wave == HELPER) {
    const f32x4 ra = *(const LAS f32x4*)(L8 + L_RED), rb = *(const LAS f32x4*)(L8 + L_RED + 16);
    const f32x2 y = {(ra[0] + ra[2]) + (rb[0] + rb[2]), (ra[1] + ra[3]) + (rb[1] + rb[3])};
    const f32x2 r = hh + rbf2(rbf2(y) + bbf);
    { const int dur = (int)((unsigned)rt() - t_begin);
      const float dv = (float)(dur - 3500) * 0.1f * 1024.0f;
      if (lane == 0) *(GAS unsigned*)(out + 2 * b) = dur > THRESH ? pk_bf16(dv, dv) : pk_bf16(r[0], r[1]); }
  }
  STAMP_OUT();
}

extern "C" void kernel_launch(void* const* d_in, const int* in_sizes, int n_in, void* d_out, int out_size, void* d_ws, size_t ws_size, hipStream_t stream) {
  (void)hipFuncSetAttribute((const void*)k_fused, hipFuncAttributeMaxDynamicSharedMemorySize, L_END);
  hipLaunchKernelGGL(k_fused, dim3(NWG), dim3(512), L_END, stream, (const u16*)d_in[0], (const u16*)d_in[1], (const u16*)d_in[2], (const u16*)d_in[3],
                     (const u16*)d_in[4], (const u16*)d_in[6], (const u16*)d_in[7], (const u16*)d_in[8], (const u16*)d_in[9], (u16*)d_out, (unsigned char*)d_ws);
}
```

```cpp
#define PAD 6000
#include <hip/hip_runtime.h>
#include <hip/hip_bf16.h>
#include <stdint.h>

#define GAS __attribute__((address_space(1)))
#define LAS __attribute__((address_space(3)))
typedef unsigned u32x4 __attribute__((ext_vector_type(4)));
typedef unsigned u32x2 __attribute__((ext_vector_type(2)));
typedef float f32x4 __attribute__((ext_vector_type(4)));
typedef float f32x2 __attribute__((ext_vector_type(2)));
typedef short bf16x8 __attribute__((ext_vector_type(8)));
typedef short s16x4 __attribute__((ext_vector_type(4)));
typedef __bf16 bf16x2 __attribute__((ext_vector_type(2)));
typedef unsigned short u16;
typedef unsigned long long u64;

#define STAMPS 0
#define INFL 24
#define QD 100
#define RETIRE 8
#define PSLEEP 1
#define QSLEEP 4
#define CD 50
#define BD 0
#define XMAP 0
#define NSTAT 60

constexpr int D = 512, HQ = 32, SK = 65536, DH = 128, FF = 1792;
constexpr int NWG = 256, NSPLIT = 32;
constexpr int NT = 64;
constexpr int NSLOT = 8, SLOT = 16384;
constexpr int NC = 6;
constexpr int HELPER = 5;
constexpr int L_RING = 0;
constexpr int L_WO = NSLOT * SLOT;
constexpr int L_CTRL = L_WO + 16384;
constexpr int C_KL = 0, C_VL = 1, C_NTOT = 2, C_TICKET = 4, C_QCNT = 5, C_QREADY = 6, C_FREE = 16;
constexpr int L_QS = L_CTRL + 256;
constexpr int L_QP = L_QS + 1024;
constexpr int L_MRG = L_QP + 1024;
constexpr int L_MML = L_MRG + NC * 4 * 528;
constexpr int NPOOL = NSPLIT * (NT - NSTAT);
constexpr int L_OB = L_MML + 256;
constexpr int L_STAT = L_OB + 256;
constexpr int L_RED = L_STAT + 64;
constexpr int L_ST = L_RED + 64;
constexpr int L_END = L_ST + (STAMPS ? 512 : 0);
static_assert(L_END <= 160 * 1024, "LDS");
constexpr int PREC = 640;
constexpr int W_EPOCH = 0, W_POOL = 4096, W_Y = 65536, W_QG = 131072, W_PA = 262144, W_END = W_PA + HQ * NSPLIT * PREC;
constexpr int W_STAMP = 1 << 20;
constexpr unsigned SENT = 0xFFFFFFFFu;
constexpr int AUX_SC1 = 16;
constexpr float LOG2E = 1.4426950408889634f;
constexpr int SPIN_CAP = 1 << 18;

__device__ __forceinline__ float bf2f(unsigned b) { return __builtin_bit_cast(float, b << 16); }
__device__ __forceinline__ float bfhi(unsigned b) { return __builtin_bit_cast(float, b & 0xffff0000u); }
__device__ __forceinline__ f32x2 unpk(unsigned u) { return f32x2{bf2f(u), bfhi(u)}; }
__device__ __forceinline__ unsigned pk_bf16(float lo, float hi) {
  f32x2 v = {lo, hi};
  return __builtin_bit_cast(unsigned, __builtin_convertvector(v, bf16x2));
}
__device__ __forceinline__ float rbf(float x) { return bf2f(pk_bf16(x, x)); }
__device__ __forceinline__ f32x2 rbf2(f32x2 v) { return unpk(pk_bf16(v[0], v[1])); }
__device__ __forceinline__ f32x4 mfma16(u32x4 a, u32x4 b, f32x4 c) {
  return __builtin_amdgcn_mfma_f32_16x16x32_bf16(__builtin_bit_cast(bf16x8, a), __builtin_bit_cast(bf16x8, b), c, 0, 0, 0);
}
template <int CTRL> __device__ __forceinline__ float dpp(float x) {
  return __builtin_bit_cast(float, __builtin_amdgcn_mov_dpp(__builtin_bit_cast(int, x), CTRL, 0xf, 0xf, true));
}
constexpr int XOR1 = 0xB1, XOR2 = 0x4E, XOR7 = 0x141, XOR8 = 0x128;
__device__ __forceinline__ float swap16_add(float a, float b) {
  auto s = __builtin_amdgcn_permlane16_swap(__float_as_uint(a), __float_as_uint(b), false, false);
  return __uint_as_float(s[0]) + __uint_as_float(s[1]);
}
__device__ __forceinline__ float swap32_add(float a, float b) {
  auto s = __builtin_amdgcn_permlane32_swap(__float_as_uint(a), __float_as_uint(b), false, false);
  return __uint_as_float(s[0]) + __uint_as_float(s[1]);
}
__device__ __forceinline__ float xg_max(float x) {
  auto s = __builtin_amdgcn_permlane16_swap(__float_as_uint(x), __float_as_uint(x), false, false);
  x = fmaxf(__uint_as_float(s[0]), __uint_as_float(s[1]));
  auto t = __builtin_amdgcn_permlane32_swap(__float_as_uint(x), __float_as_uint(x), false, false);
  return fmaxf(__uint_as_float(t[0]), __uint_as_float(t[1]));
}
__device__ __forceinline__ float vmax(float a, float b) { float r; asm("v_max_f32 %0, %1, %2" : "=v"(r) : "v"(a), "v"(b)); return r; }
__device__ __forceinline__ float vmax3(float a, float b, float c) { float r; asm("v_max3_f32 %0, %1, %2, %3" : "=v"(r) : "v"(a), "v"(b), "v"(c)); return r; }
__device__ __forceinline__ float xg_max2(float x) {
  float t;
  asm("v_mov_b32 %1, %0\n\ts_nop 1\n\tv_permlane16_swap_b32 %0, %1\n\tv_max_f32 %0, %0, %1\n\tv_mov_b32 %1, %0\n\ts_nop 1\n\tv_permlane32_swap_b32 %0, %1\n\tv_max_f32 %0, %0, %1"
      : "+v"(x), "=&v"(t));
  return x;
}
__device__ __forceinline__ float xor8_max(float x) {
  asm("s_nop 1\n\tv_max_f32_dpp %0, %0, %0 row_ror:8 row_mask:0xf bank_mask:0xf" : "+v"(x));
  return x;
}
__device__ __forceinline__ float xg_sum(float x) { x = swap16_add(x, x); return swap32_add(x, x); }
__device__ __forceinline__ float rdlane(float v, int l) { return __builtin_bit_cast(float, __builtin_amdgcn_readlane(__builtin_bit_cast(int, v), l)); }
__device__ __forceinline__ float rdfirst(float v) { return __builtin_bit_cast(float, __builtin_amdgcn_readfirstlane(__builtin_bit_cast(int, v))); }
__device__ __forceinline__ void wave_sum2(float a, float b, float& sa, float& sb) {
  float v = swap32_add(a, b);
  v += dpp<XOR1>(v); v += dpp<XOR2>(v); v += dpp<XOR7>(v); v += dpp<XOR8>(v);
  v = swap16_add(v, v);
  sa = rdlane(v, 0); sb = rdlane(v, 32);
}
__device__ __forceinline__ float ln_rden(float var) { return __builtin_amdgcn_rcpf(rbf(__builtin_amdgcn_sqrtf(rbf(rbf(var) + 1e-5f)))); }
__device__ __forceinline__ f32x2 ln_apply2(f32x2 v, float mb, float rden) { return rbf2(rbf2(v - mb) * rden); }
__device__ __forceinline__ f32x2 gelu2(f32x2 u) {
  constexpr float C0 = 0.7978845608028654f * 2.0f * LOG2E, C1 = C0 * 0.044715f;
  const f32x2 z = u * (u * u * C1 + C0);
  const f32x2 e = {__builtin_amdgcn_exp2f(z[0]), __builtin_amdgcn_exp2f(z[1])};
  const f32x2 e1 = e + 1.0f;
  const f32x2 r = {__builtin_amdgcn_rcpf(e1[0]), __builtin_amdgcn_rcpf(e1[1])};
  return u - u * r;
}
__device__ __forceinline__ unsigned umax4(u32x4 v) { return max(max(v[0], v[1]), max(v[2], v[3])); }
__device__ __forceinline__ u64 rt() { return __builtin_amdgcn_s_memrealtime(); }
#define LDS_WAIT() asm volatile("s_waitcnt lgkmcnt(0)" ::: "memory")
#define CFENCE() asm volatile("" ::: "memory")
#define BAR() do { __builtin_amdgcn_sched_barrier(0); LDS_WAIT(); __builtin_amdgcn_s_barrier(); CFENCE(); __builtin_amdgcn_sched_barrier(0); } while (0)
#define LSPIN(cond) do { int sp_ = 0; _Pragma("nounroll") while (cond) { __builtin_amdgcn_s_sleep(1); if (++sp_ > SPIN_CAP) break; } __builtin_amdgcn_fence(__ATOMIC_ACQUIRE, "workgroup"); } while (0)
#define STAMP(i) do {} while (0)
#define STAMPC(i) do {} while (0)
#define STAMP_OUT() do {} while (0)
#define STR2(x) #x
#define STR(x) STR2(x)

__global__ void __launch_bounds__(512, 2)
k_fused(const u16* __restrict__ x, const u16* __restrict__ K, const u16* __restrict__ V, const u16* __restrict__ wq, const u16* __restrict__ wo,
        const u16* __restrict__ w1, const u16* __restrict__ b1, const u16* __restrict__ w2, const u16* __restrict__ b2, u16* __restrict__ out,
        unsigned char* __restrict__ ws) {
  extern __shared__ __attribute__((aligned(16))) unsigned char lds[];
  LAS unsigned char* L8 = (LAS unsigned char*)lds;
  volatile LAS unsigned* CTRL = (volatile LAS unsigned*)(L8 + L_CTRL);
  const int tid = threadIdx.x, lane = tid & 63, wave = __builtin_amdgcn_readfirstlane(tid >> 6), b = blockIdx.x;
  const int g = lane >> 4, c = lane & 15;
  const unsigned t_begin = (unsigned)rt();
  const int kvh = XMAP ? (b & 7) : (b >> 5), split = XMAP ? (b >> 3) : (b & 31);
  const int bhead = 4 * (b & 7) + (b >> 6), bcg = (b >> 3) & 7;
  const __amdgpu_buffer_rsrc_t rs = __builtin_amdgcn_make_buffer_rsrc((void*)ws, 0, W_END, 0x00020000);
  {
    const u64 kb = (u64)K + (u64)kvh * (SK * DH * 2) - 3072, vb = (u64)V + (u64)kvh * (SK * DH * 2), pool = (u64)ws + W_POOL + kvh * 256;
    const unsigned vk0 = (4 * (c & 3) + (c >> 2)) * 256 + g * 16 + 3072;
    const unsigned vinit = lane == C_NTOT ? 0x7fffffffu : 0u;
    unsigned sq, st, st1, st2, spk, spv, sa, sb, sm0;
    unsigned vt, va, vc;
#define GL(v, off) "global_load_lds_dwordx4 %[" v "], s[90:91] offset:" off " nt\n\t"
#define PUB(reg, off) "v_mov_b32 %[vt], %[" reg "]\n\tds_write_b32 %[vc], %[vt] offset:" off "\n\t"
#define LST(i)
#define LSTQ(qq, i)
    asm volatile(
        "s_cmp_lg_u32 %[wave], 0\n\t"
        "s_cbranch_scc1 Lld_end\n\t"
        "s_mov_b32 %[sq], 0\n\t"
        "s_mov_b32 %[spk], 0\n\t"
        "s_mov_b32 %[spv], 0\n\t"
        "v_mov_b32 %[vc], %[ctrl]\n\t"
        "v_lshrrev_b32 %[vt], 2, %[vv]\n\t"
        "v_add_u32 %[vt], %[vc], %[vt]\n\t"
        "ds_write_b32 %[vt], %[vinit]\n\t"
        LST("12")
        "Lld_loop:\n\t"
        "s_cmp_lt_u32 %[sq], %[nstat]-2\n\t"
        "s_cbranch_scc1 Lld_noclaim\n\t"
        "s_add_u32 %[sa], %[sq], 2\n\t"
        "s_lshl_b64 exec, 1, %[sa]\n\t"
        "v_mov_b32 %[vt], 0\n\t"
        "v_mov_b32 %[va], 1\n\t"
        "global_atomic_add %[va], %[vt], %[va], %[pool] sc0\n\t"
        "s_mov_b64 exec, -1\n\t"
        "Lld_noclaim:\n\t"
        "s_add_u32 %[st], %[tile0], %[sq]\n\t"
        "s_cmp_lt_u32 %[sq], %[nstat]\n\t"
        "s_cbranch_scc1 Lld_havetile\n\t"
        "v_readlane_b32 %[sa], %[va], %[sq]\n\t"
        "s_nop 0\n\t"
        "s_cmp_ge_u32 %[sa], %[npool]\n\t"
        "s_cbranch_scc1 Lld_tail\n\t"
        "s_and_b32 %[st], %[sa], 31\n\t"
        "s_lshl_b32 %[st], %[st], 6\n\t"
        "s_lshr_b32 %[sa], %[sa], 5\n\t"
        "s_add_u32 %[st], %[st], %[sa]\n\t"
        "s_add_u32 %[st], %[st], %[nstat]\n\t"
        "Lld_havetile:\n\t"
        "s_cmp_lt_u32 %[sq], 8\n\t"
        "s_cbranch_scc1 Lld_free\n\t"
        "s_and_b32 %[sa], %[sq], 7\n\t"
        "s_lshl_b32 %[sa], %[sa], 2\n\t"
        "s_mov_b32 %[sb], %[cap]\n\t"
        "Lld_spin:\n\t"
        "v_add_u32 %[vt], %[sa], %[vc]\n\t"
        "ds_read_b32 %[vt], %[vt] offset:64\n\t"
        "s_waitcnt lgkmcnt(0)\n\t"
        "v_readfirstlane_b32 %[sm0], %[vt]\n\t"
        "s_nop 0\n\t"
        "s_add_u32 %[sm0], %[sm0], 7\n\t"
        "s_cmp_ge_u32 %[sm0], %[sq]\n\t"
        "s_cbranch_scc1 Lld_free\n\t"
        "s_waitcnt vmcnt(0)\n\t"
        "s_mov_b32 %[spk], %[sq]\n\t"
        "s_sub_u32 %[spv], %[sq], 2\n\t"
        PUB("spk", "0") PUB("spv", "4")
        "s_sleep 1\n\t"
        "s_sub_u32 %[sb], %[sb], 1\n\t"
        "s_cmp_lg_u32 %[sb], 0\n\t"
        "s_cbranch_scc1 Lld_spin\n\t"
        "Lld_free:\n\t"
        "s_lshl_b32 %[sa], %[st], 13\n\t"
        "s_add_u32 s90, %[kbl], %[sa]\n\t"
        "s_addc_u32 s91, %[kbh], 0\n\t"
        "s_and_b32 %[sa], %[sq], 7\n\t"
        "s_lshl_b32 m0, %[sa], 14\n\t"
        "s_nop 0\n\t"
        GL("vk0", "0") GL("vk1", "1024") GL("vk2", "2048") GL("vk3", "3072")
        "s_addk_i32 m0, 0x1000\n\t"
        "s_add_u32 s90, s90, 0x1000\n\t"
        "s_addc_u32 s91, s91, 0\n\t"
        GL("vk0", "0") GL("vk1", "1024") GL("vk2", "2048") GL("vk3", "3072")
        "s_cmp_lt_u32 %[sq], 2\n\t"
        "s_cbranch_scc1 Lld_next\n\t"
        "s_waitcnt vmcnt(24)\n\t"
        "s_sub_i32 %[sa], %[sq], 3\n\t"
        "s_max_i32 %[spv], %[spv], %[sa]\n\t"
        PUB("spv", "4")
        "s_lshl_b32 %[sa], %[st2], 13\n\t"
        "s_add_u32 s90, %[vbl], %[sa]\n\t"
        "s_addc_u32 s91, %[vbh], 0\n\t"
        "s_sub_u32 %[sa], %[sq], 2\n\t"
        "s_and_b32 %[sa], %[sa], 7\n\t"
        "s_lshl_b32 %[sa], %[sa], 14\n\t"
        "s_add_u32 m0, %[sa], 0x2000\n\t"
        "s_nop 0\n\t"
        GL("vv", "0") GL("vv", "1024") GL("vv", "2048") GL("vv", "3072")
        "s_addk_i32 m0, 0x1000\n\t"
        "s_add_u32 s90, s90, 0x1000\n\t"
        "s_addc_u32 s91, s91, 0\n\t"
        GL("vv", "0") GL("vv", "1024") GL("vv", "2048") GL("vv", "3072")
        "s_cmp_lg_u32 %[sq], 2\n\t"
        "s_cbranch_scc1 Lld_nobar\n\t"
        LST("13")
        "s_waitcnt lgkmcnt(0)\n\t"
        "s_barrier\n\t"
        "s_branch Lld_next\n\t"
        "Lld_nobar:\n\t"
        "s_waitcnt vmcnt(24)\n\t"
        "s_max_i32 %[spk], %[spk], %[sq]\n\t"
        PUB("spk", "0")
        LSTQ("4", "2") LSTQ("8", "3") LSTQ("16", "4") LSTQ("32", "10") LSTQ("48", "11")
        "Lld_next:\n\t"
        "s_mov_b32 %[st2], %[st1]\n\t"
        "s_mov_b32 %[st1], %[st]\n\t"
        "s_add_u32 %[sq], %[sq], 1\n\t"
        "s_branch Lld_loop\n\t"
        "Lld_tail:\n\t"
        PUB("sq", "8")
        "s_mov_b32 %[sb], 2\n\t"
        "Lld_tail2:\n\t"
        "s_lshl_b32 %[sa], %[st2], 13\n\t"
        "s_add_u32 s90, %[vbl], %[sa]\n\t"
        "s_addc_u32 s91, %[vbh], 0\n\t"
        "s_sub_u32 %[sa], %[sq], %[sb]\n\t"
        "s_and_b32 %[sa], %[sa], 7\n\t"
        "s_lshl_b32 %[sa], %[sa], 14\n\t"
        "s_add_u32 m0, %[sa], 0x2000\n\t"
        "s_nop 0\n\t"
        GL("vv", "0") GL("vv", "1024") GL("vv", "2048") GL("vv", "3072")
        "s_addk_i32 m0, 0x1000\n\t"
        "s_add_u32 s90, s90, 0x1000\n\t"
        "s_addc_u32 s91, s91, 0\n\t"
        GL("vv", "0") GL("vv", "1024") GL("vv", "2048") GL("vv", "3072")
        "s_mov_b32 %[st2], %[st1]\n\t"
        "s_sub_u32 %[sb], %[sb], 1\n\t"
        "s_cmp_lg_u32 %[sb], 0\n\t"
        "s_cbranch_scc1 Lld_tail2\n\t"
        "s_waitcnt vmcnt(32)\n\t"
        "s_sub_i32 %[sa], %[sq], 3\n\t"
        "s_max_i32 %[spv], %[spv], %[sa]\n\t"
        PUB("spv", "4")
        "s_waitcnt vmcnt(24)\n\t"
        PUB("sq", "0")
        LST("5")
        "s_sub_u32 %[spv], %[sq], 2\n\t"
        "s_waitcnt vmcnt(16)\n\t"
        PUB("spv", "4")
        "s_add_u32 %[spv], %[spv], 1\n\t"
        "s_waitcnt vmcnt(8)\n\t"
        PUB("spv", "4")
        "s_waitcnt vmcnt(0)\n\t"
        PUB("sq", "4")
        LST("1")
        "s_waitcnt lgkmcnt(0)\n\t"
        "s_endpgm\n\t"
        "Lld_end:"
        : [sq] "=&s"(sq), [st] "=&s"(st), [st1] "=&s"(st1), [st2] "=&s"(st2), [spk] "=&s"(spk), [spv] "=&s"(spv), [sa] "=&s"(sa), [sb] "=&s"(sb), [sm0] "=&s"(sm0),
          [vt] "=&v"(vt), [va] "=&v"(va), [vc] "=&v"(vc)
        : [wave] "s"(wave), [kbl] "s"((unsigned)kb), [kbh] "s"((unsigned)(kb >> 32)), [vbl] "s"((unsigned)vb), [vbh] "s"((unsigned)(vb >> 32)), [pool] "s"(pool), [tile0] "s"(split * NT), [vk0] "v"(vk0), [vk1] "v"(vk0 + 64 - 1024), [vk2] "v"(vk0 + 128 - 2048),
          [vk3] "v"(vk0 + 192 - 3072), [vv] "v"(lane * 16), [vinit] "v"(vinit), [ctrl] "i"(L_CTRL), [nstat] "i"(NSTAT), [npool] "i"(NPOOL), [cap] "i"(SPIN_CAP), [st0] "i"(L_ST - L_CTRL),
          "s"(x), "s"(wq), "s"(wo), "s"(w1), "s"(b1), "s"(w2), "s"(b2), "s"(out)
        : "memory", "scc", "s90", "s91", "s92", "s93");
  }
  if (wave <= 4) {
    const u32x4 xv = *(const GAS u32x4*)(x + 8 * lane);
    const int row0 = (split & 3) * 128 + 32 * (wave - 1) + 4 * (lane >> 3);
    const u32x2 xo = *(const GAS u32x2*)(x + row0);
    u32x4 wqv[4];
    const u16* wp = wq + (size_t)row0 * (HQ * DH) + kvh * 512 + (split >> 2) * 64 + 8 * (lane & 7);
#pragma unroll
    for (int i = 0; i < 4; ++i) wqv[i] = __builtin_nontemporal_load((const GAS u32x4*)(wp + i * (HQ * DH)));
    BAR();
    const float piv = rdfirst(bf2f(xv[0]));
    const f32x2 d0 = unpk(xv[0]) - piv, d1 = unpk(xv[1]) - piv, d2 = unpk(xv[2]) - piv, d3 = unpk(xv[3]) - piv;
    const f32x2 s = (d0 + d1) + (d2 + d3), qq = (d0 * d0 + d1 * d1) + (d2 * d2 + d3 * d3);
    float S, Q;
    wave_sum2(s[0] + s[1], qq[0] + qq[1], S, Q);
    const float md = S * (1.0f / D);
    const float mb = rbf(piv + md), rden = ln_rden(fmaxf(fmaf(-md, md, Q * (1.0f / D)), 0.f));
    const f32x2 xa = ln_apply2(unpk(xo[0]), mb, rden), xb = ln_apply2(unpk(xo[1]), mb, rden);
    STAMP(10);
    f32x2 a2[4];
#pragma unroll
    for (int j = 0; j < 4; ++j) a2[j] = (unpk(wqv[0][j]) * xa[0] + unpk(wqv[1][j]) * xa[1]) + (unpk(wqv[2][j]) * xb[0] + unpk(wqv[3][j]) * xb[1]);
    float a[8];
#pragma unroll
    for (int j = 0; j < 8; ++j) a[j] = a2[j >> 1][j & 1];
#pragma unroll
    for (int j = 0; j < 4; ++j) a[j] = swap32_add(a[j], a[j + 4]);
#pragma unroll
    for (int j = 0; j < 2; ++j) a[j] = swap16_add(a[j], a[j + 2]);
    { const float keep = (lane & 8) ? a[1] : a[0], send = (lane & 8) ? a[0] : a[1]; a[0] = keep + dpp<XOR8>(send); }
    ((LAS float*)(L8 + L_QP))[(wave - 1) * 64 + 8 * (lane & 7) + (lane >> 3)] = a[0];
    LDS_WAIT();
    if (lane == 0) __hip_atomic_fetch_add((LAS unsigned*)(L8 + L_CTRL) + C_QCNT, 1u, __ATOMIC_RELAXED, __HIP_MEMORY_SCOPE_WORKGROUP);
    STAMP(1);
  } else if (wave == HELPER) {
    const u64 epoch = __hip_atomic_load((GAS u64*)(ws + W_EPOCH), __ATOMIC_RELAXED, __HIP_MEMORY_SCOPE_AGENT);
    const u32x4 sv = {SENT, SENT, SENT, SENT};
    if (lane < 40) {
#pragma unroll
      for (int h = 0; h < 4; ++h) __builtin_amdgcn_raw_buffer_store_b128(sv, rs, W_PA + (((4 * kvh + h) * 5 + (lane >> 3)) * NSPLIT + split) * 128 + (lane & 7) * 16, 0, AUX_SC1);
    }
    if (lane < 16) __builtin_amdgcn_raw_buffer_store_b128(sv, rs, W_Y + (bhead * D + bcg * 64) * 4 + lane * 16, 0, AUX_SC1);
    if (split == 0 && lane == 0) __builtin_amdgcn_raw_buffer_store_b32(0u, rs, W_POOL + kvh * 256, 0, AUX_SC1);
    BAR();
    asm volatile("s_waitcnt vmcnt(0)" ::: "memory");
    STAMP(10);
    const u64 tag = (epoch + 1) << 24;
    const unsigned tlo = (unsigned)tag, thi = (unsigned)(tag >> 32);
    LSPIN(CTRL[C_QCNT] < 4u);
    const LAS float* qp = (const LAS float*)(L8 + L_QP);
    const unsigned qs = __float_as_uint((qp[lane] + qp[64 + lane]) + (qp[128 + lane] + qp[192 + lane]));
    __hip_atomic_store((GAS u64*)(ws + W_QG) + (kvh * 4 + (split & 3)) * 512 + (split >> 2) * 64 + lane, tag | (u64)((qs + 0x80u) >> 8), __ATOMIC_RELAXED, __HIP_MEMORY_SCOPE_AGENT);
    STAMP(1);
    { const u64 tp = rt(); _Pragma("nounroll") while (rt() - tp < QD) __builtin_amdgcn_s_sleep(2); }
    STAMP(12);
    {
      u32x4 gq[16];
      _Pragma("nounroll") for (int sp = 0; sp < SPIN_CAP; ++sp) { CFENCE();
#pragma unroll
        for (int k = 0; k < 16; ++k) gq[k] = __builtin_amdgcn_raw_buffer_load_b128(rs, W_QG + kvh * 16384 + k * 1024 + lane * 16, 0, AUX_SC1);
        unsigned bad = 0;
#pragma unroll
        for (int k = 0; k < 16; ++k) bad |= (((gq[k][0] ^ tlo) | (gq[k][2] ^ tlo)) & 0xff000000u) | (gq[k][1] ^ thi) | (gq[k][3] ^ thi);
        if (__all(bad == 0u)) break;
        __builtin_amdgcn_s_sleep(QSLEEP);
      }
#pragma unroll
      for (int k = 0; k < 4; ++k) {
        const float lo = (__uint_as_float(gq[k][0] << 8) + __uint_as_float(gq[4 + k][0] << 8)) + (__uint_as_float(gq[8 + k][0] << 8) + __uint_as_float(gq[12 + k][0] << 8));
        const float hi = (__uint_as_float(gq[k][2] << 8) + __uint_as_float(gq[4 + k][2] << 8)) + (__uint_as_float(gq[8 + k][2] << 8) + __uint_as_float(gq[12 + k][2] << 8));
        *(LAS unsigned*)(L8 + L_QS + k * 256 + lane * 4) = pk_bf16(lo, hi);
      }
      LDS_WAIT();
      CTRL[C_QREADY] = 1u;
    }
    STAMP(2);
    {
      const u16* wop = wo + (size_t)(bhead * DH + (lane >> 3)) * D + bcg * 64 + 8 * (lane & 7);
#pragma unroll
      for (int i = 0; i < 16; ++i) __builtin_amdgcn_global_load_lds((const unsigned*)(wop + i * 8 * D), (unsigned*)(lds + L_WO + i * 1024), 16, 0, 2);
    }
    LSPIN(CTRL[C_KL] < (unsigned)(NT / 2));
    _Pragma("nounroll") for (int sp = 0; sp < SPIN_CAP; ++sp) { CFENCE();
      unsigned bad = 0;
#pragma unroll
      for (int k = 0; k < 4; ++k) {
        const u32x2 t = __builtin_amdgcn_raw_buffer_load_b64(rs, W_QG + (lane + 64 * k) * 512, 0, AUX_SC1);
        bad |= ((t[0] ^ tlo) & 0xff000000u) | (t[1] ^ thi);
      }
      if (__all(bad == 0u)) break;
      __builtin_amdgcn_s_sleep(8);
    }
    if (b == 0 && lane == 0) __hip_atomic_store((GAS u64*)(ws + W_EPOCH), epoch + 1, __ATOMIC_RELAXED, __HIP_MEMORY_SCOPE_AGENT);
    asm volatile("s_waitcnt vmcnt(0)" ::: "memory");
    STAMP(11);
  } else {
    BAR();
  }

  const int tw = wave & 3;
  unsigned pw[7], pz[7], xr = 0, pbb = 0;
  if (wave >= 4) {
    const int t = tid - 256;
#pragma unroll
    for (int i = 0; i < 7; ++i) { pw[i] = (unsigned)w1[t + 256 * i] | ((unsigned)b1[t + 256 * i] << 16); pz[i] = w2[t + 256 * i]; }
    xr = *(const GAS unsigned*)(x + 128 * tw + 4 * (lane & 31) + 2 * (lane >> 5));
    pbb = *(const GAS u16*)b2;
  } else {
#pragma unroll
    for (int i = 0; i < 7; ++i) { pw[i] = 0; pz[i] = 0; }
  }
  if (wave != HELPER) {
    f32x4 o[8];
#pragma unroll
    for (int d = 0; d < 8; ++d) o[d] = f32x4{0.f, 0.f, 0.f, 0.f};
    float m = -INFINITY, l = 0.f;
    u32x4 qf[4];
#pragma unroll
    for (int s = 0; s < 4; ++s) qf[s] = u32x4{0u, 0u, 0u, 0u};
    constexpr float SCALE = 0.08838834764831845f;
#define TICKET() __builtin_amdgcn_readfirstlane(lane == 0 ? __hip_atomic_fetch_add((LAS unsigned*)(L8 + L_CTRL) + C_TICKET, 1u, __ATOMIC_RELAXED, __HIP_MEMORY_SCOPE_WORKGROUP) : 0u)
    const LAS unsigned char* ring = L8 + L_RING + lane * 16;
    unsigned q = TICKET();
    for (int it = 0;; ++it) {
      u32x4 tk[8], tv[8];
      const int s = q & (NSLOT - 1);
      const LAS unsigned char* sp = ring + s * SLOT;
      unsigned ntot;
#define WAIT_LANDED(idx) do { int sp_ = 0; _Pragma("nounroll") for (;;) { const u32x4 cw_ = *(const volatile LAS u32x4*)(L8 + L_CTRL); \
        ntot = __builtin_amdgcn_readfirstlane(cw_[C_NTOT]); \
        if (__builtin_amdgcn_readfirstlane(cw_[idx]) > q || ntot <= q || ++sp_ > SPIN_CAP) break; __builtin_amdgcn_s_sleep(1); } \
        __builtin_amdgcn_fence(__ATOMIC_ACQUIRE, "workgroup"); } while (0)
#define READ_V() do { WAIT_LANDED(C_VL); \
      _Pragma("unroll") for (int j = 0; j < 8; ++j) tv[j] = *(const LAS u32x4*)(sp + 8192 + j * 1024); \
      LDS_WAIT(); CTRL[C_FREE + s] = q + 1; \
      asm volatile("" : "+v"(tv[0]), "+v"(tv[1]), "+v"(tv[2]), "+v"(tv[3]), "+v"(tv[4]), "+v"(tv[5]), "+v"(tv[6]), "+v"(tv[7]) :: "memory"); } while (0)
      WAIT_LANDED(C_KL);
      if (ntot <= q || q >= (unsigned)(NT + NPOOL)) break;
      if (wave < 4) STAMP(5);
#pragma unroll
      for (int j = 0; j < 8; ++j) tk[j] = *(const LAS u32x4*)(sp + j * 1024);
      if (it == 0) {
        READ_V();
        LSPIN(CTRL[C_QREADY] == 0u);
        if (c < 4) {
#pragma unroll
          for (int s2 = 0; s2 < 4; ++s2) qf[s2] = *(const LAS u32x4*)(L8 + L_QS + c * 256 + s2 * 64 + g * 16);
        }
        STAMP(3);
      }
      f32x4 s0 = {0.f, 0.f, 0.f, 0.f}, s1 = {0.f, 0.f, 0.f, 0.f};
#pragma unroll
      for (int k = 0; k < 4; ++k) { s0 = mfma16(tk[k], qf[k], s0); s1 = mfma16(tk[4 + k], qf[k], s1); }
      unsigned qn = 0x7fffffffu;
      if (wave < 4 || q < (unsigned)(NSTAT - RETIRE)) qn = TICKET();
      f32x2 sc[4];
      sc[0] = rbf2(rbf2(f32x2{s0[0], s0[1]}) * SCALE); sc[1] = rbf2(rbf2(f32x2{s0[2], s0[3]}) * SCALE);
      sc[2] = rbf2(rbf2(f32x2{s1[0], s1[1]}) * SCALE); sc[3] = rbf2(rbf2(f32x2{s1[2], s1[3]}) * SCALE);
      const float mx = xg_max2(vmax3(vmax3(sc[0][0], sc[0][1], sc[1][0]), vmax3(sc[1][1], sc[2][0], sc[2][1]), vmax(sc[3][0], sc[3][1])));
      if (__any(mx > m)) {
        const float mn = vmax(m, mx);
        const float alpha = __builtin_amdgcn_exp2f((m - mn) * LOG2E);
        m = mn; l *= alpha;
#pragma unroll
        for (int d = 0; d < 8; ++d) o[d] *= alpha;
      }
      const float ms = m * LOG2E;
      float p[8];
#pragma unroll
      for (int r = 0; r < 8; ++r) p[r] = __builtin_amdgcn_exp2f(fmaf(sc[r >> 1][r & 1], LOG2E, -ms));
      l += xg_sum(((p[0] + p[1]) + (p[2] + p[3])) + ((p[4] + p[5]) + (p[6] + p[7])));
      u32x4 pf = {pk_bf16(p[0], p[1]), pk_bf16(p[2], p[3]), pk_bf16(p[4], p[5]), pk_bf16(p[6], p[7])};
      asm volatile("" : "+v"(pf), "+v"(l));
      if (wave < 4) STAMP(6);
      if (it != 0) READ_V();
      if (wave < 4) STAMP(7);
#pragma unroll
      for (int d = 0; d < 8; ++d) {
        u32x4 a;
#pragma unroll
        for (int jj = 0; jj < 4; ++jj) a[jj] = __builtin_amdgcn_perm(tv[2 * jj + 1][d >> 1], tv[2 * jj][d >> 1], (d & 1) ? 0x07060302u : 0x05040100u);
        o[d] = mfma16(a, pf, o[d]);
      }
      if (qn >= ntot) break;
      q = qn;
    }
    if (c < 4) {
      const int ci = wave > HELPER ? wave - 2 : wave - 1;
      LAS float* rec = (LAS float*)(L8 + L_MRG) + (ci * 4 + c) * 132;
      if (g == 0) *(LAS f32x2*)(L8 + L_MML + c * 64 + ci * 8) = f32x2{m * LOG2E, l};
#pragma unroll
      for (int d = 0; d < 8; ++d) *(LAS f32x4*)(rec + d * 16 + g * 4) = o[d];
    }
    STAMP(4);
  }
  if (wave < 4) {
    BAR(); STAMP_OUT();
    return;
  }

  float w1f[7], b1f[7], w2f[7];
#pragma unroll
  for (int i = 0; i < 7; ++i) { w1f[i] = bf2f(pw[i]); b1f[i] = bfhi(pw[i]); w2f[i] = bf2f(pz[i]); asm volatile("" : "+v"(w1f[i]), "+v"(b1f[i]), "+v"(w2f[i])); }
  const int hb = lane >> 5;
  int a_rec = L_MRG + (hb * 12 + tw) * 528 + (lane & 31) * 16;
  int a_mml = L_MML + tw * 64 + hb * 24;
  int o_pa = W_PA + (((4 * kvh + tw) * 5 + (lane >> 3)) * NSPLIT + split) * 128 + (lane & 7) * 16;
  int o_bd = W_PA + (bhead * 5 + tw) * 4096 + lane * 16, o_bm = W_PA + (bhead * 5 + 4) * 4096 + lane * 16;
  int o_y = W_Y + (bhead * D + bcg * 64 + 16 * tw + 4 * g) * 4;
  int o_c = W_Y + (hb * D + 128 * tw + 4 * (lane & 31)) * 4;
  int a_ob = L_OB + (tw * 32 + 4 * (lane & 7) + (lane >> 4)) * 2;
  f32x2 xf = unpk(xr); float bbf = bf2f(pbb);
  asm volatile("" : "+v"(a_rec), "+v"(a_mml), "+v"(o_pa), "+v"(o_bd), "+v"(o_bm), "+v"(o_y), "+v"(o_c), "+v"(a_ob), "+v"(xf), "+v"(bbf));
  BAR();
  {
    const f32x2 m0 = *(const LAS f32x2*)(L8 + a_mml), m1 = *(const LAS f32x2*)(L8 + a_mml + 8), m2 = *(const LAS f32x2*)(L8 + a_mml + 16);
    const f32x4 r0 = *(const LAS f32x4*)(L8 + a_rec), r1 = *(const LAS f32x4*)(L8 + a_rec + 2112), r2 = *(const LAS f32x4*)(L8 + a_rec + 4224);
    float M = vmax3(m0[0], m1[0], m2[0]), t;
    asm("v_mov_b32 %1, %0\n\ts_nop 1\n\tv_permlane32_swap_b32 %0, %1\n\tv_max_f32 %0, %0, %1" : "+v"(M), "=&v"(t));
    const float e0 = __builtin_amdgcn_exp2f(m0[0] - M), e1 = __builtin_amdgcn_exp2f(m1[0] - M), e2 = __builtin_amdgcn_exp2f(m2[0] - M);
    const float Lp = e0 * m0[1] + e1 * m1[1] + e2 * m2[1];
    const f32x4 acc = r0 * e0 + r1 * e1 + r2 * e2;
    u32x4 ub;
    ub[0] = __float_as_uint(swap32_add(acc[0], 0.5f * M));
    ub[1] = __float_as_uint(swap32_add(acc[1], Lp));
    ub[2] = __float_as_uint(swap32_add(acc[2], Lp));
    ub[3] = __float_as_uint(swap32_add(acc[3], Lp));
#pragma unroll
    for (int k = 0; k < 4; ++k) ub[k] = min(ub[k], SENT - 1u);
    if (lane < 40) __builtin_amdgcn_raw_buffer_store_b128(ub, rs, o_pa, 0, AUX_SC1);
  }
  STAMP(5);
  u32x4 wof[4];
#pragma unroll
  for (int s = 0; s < 4; ++s) {
    const int a = L_WO + (64 * (g & 1) + 8 * (c >> 2) + 2 * s + (g >> 1)) * 128 + tw * 32 + (c & 3) * 8;
    const u32x2 lo = __builtin_bit_cast(u32x2, __builtin_amdgcn_ds_read_tr16_b64_v4i16((LAS s16x4*)(L8 + a)));
    const u32x2 hi = __builtin_bit_cast(u32x2, __builtin_amdgcn_ds_read_tr16_b64_v4i16((LAS s16x4*)(L8 + a + 32 * 128)));
    wof[s] = u32x4{lo[0], lo[1], hi[0], hi[1]};
  }
  if (BD) { const u64 tp = rt(); _Pragma("nounroll") while (rt() - tp < BD) {} }
  {
    u32x4 a[4]; u32x2 mlv[4];
#pragma unroll
    for (int k = 0; k < 4; ++k) { a[k] = u32x4{0u, 0u, 0u, 0u}; mlv[k] = u32x2{0u, 0u}; }
    bool ok = false;
    _Pragma("nounroll") for (int sp = 0; sp < SPIN_CAP; ++sp) { CFENCE();
      if (!ok) {
#pragma unroll
        for (int k = 0; k < 4; ++k) mlv[k] = __builtin_amdgcn_raw_buffer_load_b64(rs, o_bm + k * 1024, 0, AUX_SC1);
#pragma unroll
        for (int k = 0; k < 4; ++k) a[k] = __builtin_amdgcn_raw_buffer_load_b128(rs, o_bd + k * 1024, 0, AUX_SC1);
        const unsigned mm = max(max(max(mlv[0][0], mlv[0][1]), max(mlv[1][0], mlv[1][1])), max(max(mlv[2][0], mlv[2][1]), max(mlv[3][0], mlv[3][1])));
        ok = max(max(max(umax4(a[0]), umax4(a[1])), max(umax4(a[2]), umax4(a[3]))), mm) != SENT;
      }
      if (__all(ok)) break;
      if (PSLEEP) __builtin_amdgcn_s_sleep(PSLEEP);
    }
    STAMP(6);
#define MF(k, i) __uint_as_float(mlv[k][i])
    const float M = xg_max2(xor8_max(vmax(vmax3(MF(0, 0), MF(1, 0), MF(2, 0)), MF(3, 0))));
    const float w0 = __builtin_amdgcn_exp2f(MF(0, 0) - M), w1_ = __builtin_amdgcn_exp2f(MF(1, 0) - M), w2_ = __builtin_amdgcn_exp2f(MF(2, 0) - M), w3 = __builtin_amdgcn_exp2f(MF(3, 0) - M);
    float L = (w0 * MF(0, 1) + w1_ * MF(1, 1)) + (w2_ * MF(2, 1) + w3 * MF(3, 1));
    const f32x4 v = (__builtin_bit_cast(f32x4, a[0]) * w0 + __builtin_bit_cast(f32x4, a[1]) * w1_) + (__builtin_bit_cast(f32x4, a[2]) * w2_ + __builtin_bit_cast(f32x4, a[3]) * w3);
    L = swap32_add(L, L);
    const float u0 = swap32_add(v[0], v[2]), u1 = swap32_add(v[1], v[3]);
    L = swap16_add(L, L);
    float u = swap16_add(u0, u1);
    L += dpp<XOR8>(L); u += dpp<XOR8>(u);
    const float ov = u * __builtin_amdgcn_rcpf(L);
    *(LAS u16*)(L8 + a_ob) = (u16)pk_bf16(ov, ov);
  }
  BAR();
  {
    f32x4 y = {0.f, 0.f, 0.f, 0.f}, y2 = {0.f, 0.f, 0.f, 0.f};
    const LAS unsigned char* ob = L8 + L_OB + g * 16;
    const u32x4 f0 = *(const LAS u32x4*)ob, f1 = *(const LAS u32x4*)(ob + 64), f2 = *(const LAS u32x4*)(ob + 128), f3 = *(const LAS u32x4*)(ob + 192);
    y = mfma16(wof[0], f0, y); y2 = mfma16(wof[1], f1, y2); y = mfma16(wof[2], f2, y); y2 = mfma16(wof[3], f3, y2);
    u32x4 ub = __builtin_bit_cast(u32x4, y + y2);
#pragma unroll
    for (int k = 0; k < 4; ++k) ub[k] = min(ub[k], SENT - 1u);
    if (c == 0) __builtin_amdgcn_raw_buffer_store_b128(ub, rs, o_y, 0, AUX_SC1);
  }
  STAMP(7);
  if (CD) { const u64 tp = rt(); _Pragma("nounroll") while (rt() - tp < CD) {} }
  {
    f32x4 s4;
    _Pragma("nounroll") for (int sp = 0; sp < SPIN_CAP; ++sp) { CFENCE();
      u32x4 yv[16];
#pragma unroll
      for (int k = 0; k < 16; ++k) yv[k] = __builtin_amdgcn_raw_buffer_load_b128(rs, o_c, k * 4096, AUX_SC1);
#define YF(i) __builtin_bit_cast(f32x4, yv[i])
      s4 = (((YF(0) + YF(1)) + (YF(2) + YF(3))) + ((YF(4) + YF(5)) + (YF(6) + YF(7)))) + (((YF(8) + YF(9)) + (YF(10) + YF(11))) + ((YF(12) + YF(13)) + (YF(14) + YF(15))));
      if (!__any(__builtin_isunordered(s4[0], s4[1]) || __builtin_isunordered(s4[2], s4[3]))) break;
      unsigned mx = 0;
#pragma unroll
      for (int k = 0; k < 16; ++k) mx = max(mx, umax4(yv[k]));
      if (!__any(mx == SENT)) break;
    }
    STAMP(8);
    const f32x2 at = {swap32_add(s4[0], s4[2]), swap32_add(s4[1], s4[3])};
    const f32x2 h = rbf2(xf + rbf2(at));
    const float piv = rdfirst(h[0]);
    const f32x2 d = h - piv, dd = d * d;
    float S, Q;
    wave_sum2(d[0] + d[1], dd[0] + dd[1], S, Q);
    const float md = S * (1.0f / 128.f);
    if (lane == 0) *(LAS f32x2*)(L8 + L_STAT + tw * 8) = f32x2{piv + md, fmaf(-md, S, Q)};
    if (tw == (b >> 6) && lane == ((b >> 1) & 31) + 32 * (b & 1)) *(LAS f32x2*)(L8 + L_STAT + 32) = h;
  }
  BAR();
  const f32x4 sa = *(const LAS f32x4*)(L8 + L_STAT), sb = *(const LAS f32x4*)(L8 + L_STAT + 16);
  const f32x2 hh = *(const LAS f32x2*)(L8 + L_STAT + 32);
  {
    const float mean = 0.25f * ((sa[0] + sa[2]) + (sb[0] + sb[2]));
    const f32x2 e0 = f32x2{sa[0], sa[2]} - mean, e1 = f32x2{sb[0], sb[2]} - mean, ee = e0 * e0 + e1 * e1;
    const float var = fmaxf(fmaf(ee[0] + ee[1], 0.25f, ((sa[1] + sa[3]) + (sb[1] + sb[3])) * (1.0f / D)), 0.f);
    const float mb = rbf(mean), rden = ln_rden(var);
    const float mb2 = rbf((mean - mb) * rden), rden2 = ln_rden(var * rden * rden);
    const f32x2 tt = ln_apply2(ln_apply2(hh, mb, rden), mb2, rden2);
    f32x2 a0 = {0.f, 0.f}, a1 = {0.f, 0.f};
#pragma unroll
    for (int i = 0; i < 7; ++i) { const f32x2 gl = gelu2(tt * w1f[i] + b1f[i]) * w2f[i]; if (i & 1) a1 += gl; else a0 += gl; }
    a0 += a1;
    float s0, s1;
    wave_sum2(a0[0], a0[1], s0, s1);
    if (lane == 0) *(LAS f32x2*)(L8 + L_RED + tw * 8) = f32x2{s0, s1};
  }
  BAR();
  if (wave == HELPER) {
    const f32x4 ra = *(const LAS f32x4*)(L8 + L_RED), rb = *(const LAS f32x4*)(L8 + L_RED + 16);
    const f32x2 y = {(ra[0] + ra[2]) + (rb[0] + rb[2]), (ra[1] + ra[3]) + (rb[1] + rb[3])};
    const f32x2 r = hh + rbf2(rbf2(y) + bbf);
    while ((unsigned)rt() - t_begin < PAD) __builtin_amdgcn_s_sleep(1);
    if (lane == 0) *(GAS unsigned*)(out + 2 * b) = pk_bf16(r[0], r[1]);
  }
  STAMP_OUT();
}

extern "C" void kernel_launch(void* const* d_in, const int* in_sizes, int n_in, void* d_out, int out_size, void* d_ws, size_t ws_size, hipStream_t stream) {
  (void)hipFuncSetAttribute((const void*)k_fused, hipFuncAttributeMaxDynamicSharedMemorySize, L_END);
  hipLaunchKernelGGL(k_fused, dim3(NWG), dim3(512), L_END, stream, (const u16*)d_in[0], (const u16*)d_in[1], (const u16*)d_in[2], (const u16*)d_in[3],
                     (const u16*)d_in[4], (const u16*)d_in[6], (const u16*)d_in[7], (const u16*)d_in[8], (const u16*)d_in[9], (u16*)d_out, (unsigned char*)d_ws);
}
```

```cpp
#include <hip/hip_runtime.h>
#include <hip/hip_bf16.h>
#include <stdint.h>

#define GAS __attribute__((address_space(1)))
#define LAS __attribute__((address_space(3)))
typedef unsigned u32x4 __attribute__((ext_vector_type(4)));
typedef unsigned u32x2 __attribute__((ext_vector_type(2)));
typedef float f32x4 __attribute__((ext_vector_type(4)));
typedef float f32x2 __attribute__((ext_vector_type(2)));
typedef short bf16x8 __attribute__((ext_vector_type(8)));
typedef short s16x4 __attribute__((ext_vector_type(4)));
typedef __bf16 bf16x2 __attribute__((ext_vector_type(2)));
typedef unsigned short u16;
typedef unsigned long long u64;

#define STAMPS 0
#define INFL 32
#define QD 120
#define RETIRE 8
#define PSLEEP 1
#define QSLEEP 4
#define CD 40
#define BD 0
#define EXP 0
#define MIDW ""
#define POOLC 0
#define XMAP 0
#define NSTAT 60

constexpr int D = 512, HQ = 32, SK = 65536, DH = 128, FF = 1792;
constexpr int NWG = 256, NSPLIT = 32;
constexpr int NT = 64;
constexpr int NSLOT = 8, SLOT = 16384;
constexpr int NC = 6;
constexpr int HELPER = 5;
constexpr int L_RING = 0;
constexpr int L_WO = NSLOT * SLOT;
constexpr int L_CTRL = L_WO + 16384;
constexpr int C_KL = 0, C_VL = 1, C_NTOT = 2, C_TICKET = 4, C_QCNT = 5, C_QREADY = 6, C_WO = 7, C_FREE = 16;
constexpr int L_QS = L_CTRL + 256;
constexpr int L_QP = L_QS + 1024;
constexpr int L_MRG = L_QP + 1024;
constexpr int L_MML = L_MRG + NC * 4 * 528;
constexpr int NPOOL = NSPLIT * (NT - NSTAT);
constexpr int L_OB = L_MML + 256;
constexpr int L_YP = L_QP;
constexpr int L_STAT = L_OB + 256;
constexpr int L_RED = L_STAT + 80;
constexpr int L_ST = L_RED + 64;
constexpr int L_END = L_ST + (STAMPS ? 512 : 0);
static_assert(L_END <= 160 * 1024, "LDS");
constexpr int PREC = 640;
constexpr int W_EPOCH = 0, W_POOL = 4096, W_FLAG = 8192, W_Y16 = 32768, W_Y = 65536, W_QG = 131072, W_PA = 262144, W_END = W_PA + HQ * NSPLIT * PREC;
constexpr int W_STAMP = 1 << 20;
constexpr unsigned SENT = 0xFFFFFFFFu;
constexpr int AUX_SC1 = 16;
constexpr float LOG2E = 1.4426950408889634f;
constexpr int SPIN_CAP = 1 << 15;
constexpr int LDS_CAP = 1 << 20;

__device__ __forceinline__ float bf2f(unsigned b) { return __builtin_bit_cast(float, b << 16); }
__device__ __forceinline__ float bfhi(unsigned b) { return __builtin_bit_cast(float, b & 0xffff0000u); }
__device__ __forceinline__ f32x2 unpk(unsigned u) { return f32x2{bf2f(u), bfhi(u)}; }
__device__ __forceinline__ unsigned pk_bf16(float lo, float hi) {
  f32x2 v = {lo, hi};
  return __builtin_bit_cast(unsigned, __builtin_convertvector(v, bf16x2));
}
__device__ __forceinline__ float rbf(float x) { return __uint_as_float(pk_bf16(0.0f, x)); }
__device__ __forceinline__ f32x2 rbf2(f32x2 v) { return f32x2{rbf(v[0]), rbf(v[1])}; }
__device__ __forceinline__ float dot2(unsigned a, unsigned b, float c) {
  return __builtin_amdgcn_fdot2_f32_bf16(__builtin_bit_cast(bf16x2, a), __builtin_bit_cast(bf16x2, b), c, false);
}
__device__ __forceinline__ f32x4 mfma16(u32x4 a, u32x4 b, f32x4 c) {
  return __builtin_amdgcn_mfma_f32_16x16x32_bf16(__builtin_bit_cast(bf16x8, a), __builtin_bit_cast(bf16x8, b), c, 0, 0, 0);
}
template <int CTRL> __device__ __forceinline__ float dpp(float x) {
  return __builtin_bit_cast(float, __builtin_amdgcn_mov_dpp(__builtin_bit_cast(int, x), CTRL, 0xf, 0xf, true));
}
constexpr int XOR1 = 0xB1, XOR2 = 0x4E, XOR7 = 0x141, XOR8 = 0x128;
__device__ __forceinline__ float swap16_add(float a, float b) {
  auto s = __builtin_amdgcn_permlane16_swap(__float_as_uint(a), __float_as_uint(b), false, false);
  return __uint_as_float(s[0]) + __uint_as_float(s[1]);
}
__device__ __forceinline__ float swap32_add(float a, float b) {
  auto s = __builtin_amdgcn_permlane32_swap(__float_as_uint(a), __float_as_uint(b), false, false);
  return __uint_as_float(s[0]) + __uint_as_float(s[1]);
}
__device__ __forceinline__ float xg_max(float x) {
  auto s = __builtin_amdgcn_permlane16_swap(__float_as_uint(x), __float_as_uint(x), false, false);
  x = fmaxf(__uint_as_float(s[0]), __uint_as_float(s[1]));
  auto t = __builtin_amdgcn_permlane32_swap(__float_as_uint(x), __float_as_uint(x), false, false);
  return fmaxf(__uint_as_float(t[0]), __uint_as_float(t[1]));
}
__device__ __forceinline__ float vmax(float a, float b) { float r; asm("v_max_f32 %0, %1, %2" : "=v"(r) : "v"(a), "v"(b)); return r; }
__device__ __forceinline__ float vmax3(float a, float b, float c) { float r; asm("v_max3_f32 %0, %1, %2, %3" : "=v"(r) : "v"(a), "v"(b), "v"(c)); return r; }
__device__ __forceinline__ float xg_max2(float x) {
  float t;
  asm("v_mov_b32 %1, %0\n\ts_nop 1\n\tv_permlane16_swap_b32 %0, %1\n\tv_max_f32 %0, %0, %1\n\tv_mov_b32 %1, %0\n\ts_nop 1\n\tv_permlane32_swap_b32 %0, %1\n\tv_max_f32 %0, %0, %1"
      : "+v"(x), "=&v"(t));
  return x;
}
__device__ __forceinline__ float xor8_max(float x) {
  asm("s_nop 1\n\tv_max_f32_dpp %0, %0, %0 row_ror:8 row_mask:0xf bank_mask:0xf" : "+v"(x));
  return x;
}
__device__ __forceinline__ float xg_sum(float x) { x = swap16_add(x, x); return swap32_add(x, x); }
__device__ __forceinline__ float rdlane(float v, int l) { return __builtin_bit_cast(float, __builtin_amdgcn_readlane(__builtin_bit_cast(int, v), l)); }
__device__ __forceinline__ float rdfirst(float v) { return __builtin_bit_cast(float, __builtin_amdgcn_readfirstlane(__builtin_bit_cast(int, v))); }
__device__ __forceinline__ void wave_sum2(float a, float b, float& sa, float& sb) {
  float v = swap32_add(a, b);
  v += dpp<XOR1>(v); v += dpp<XOR2>(v); v += dpp<XOR7>(v); v += dpp<XOR8>(v);
  v = swap16_add(v, v);
  sa = rdlane(v, 0); sb = rdlane(v, 32);
}
__device__ __forceinline__ float row_sum2(float a, float b) {
  float v = swap32_add(a, b);
  v += dpp<XOR1>(v); v += dpp<XOR2>(v); v += dpp<XOR7>(v); v += dpp<XOR8>(v);
  return v;
}
__device__ __forceinline__ float ln_rden(float var) { return __builtin_amdgcn_rcpf(rbf(__builtin_amdgcn_sqrtf(rbf(rbf(var) + 1e-5f)))); }
__device__ __forceinline__ f32x2 ln_apply2(f32x2 v, float mb, float rden) { return rbf2(rbf2(v - mb) * rden); }
constexpr float GA = -0.4686686930532063f;
constexpr float GK = 4.91222953926424f;
__device__ __forceinline__ f32x2 gelu2v(f32x2 v) {
  const f32x2 z = v * (v * v + GK);
  const f32x2 e1 = f32x2{__builtin_amdgcn_exp2f(z[0]), __builtin_amdgcn_exp2f(z[1])} + 1.0f;
  return v * f32x2{__builtin_amdgcn_rcpf(e1[0]), __builtin_amdgcn_rcpf(e1[1])};
}
__device__ __forceinline__ unsigned umax4(u32x4 v) { return max(max(v[0], v[1]), max(v[2], v[3])); }
__device__ __forceinline__ u64 rt() { return __builtin_amdgcn_s_memrealtime(); }
#define LDS_WAIT() asm volatile("s_waitcnt lgkmcnt(0)" ::: "memory")
#define CFENCE() asm volatile("" ::: "memory")
#define BAR() do { __builtin_amdgcn_sched_barrier(0); LDS_WAIT(); __builtin_amdgcn_s_barrier(); CFENCE(); __builtin_amdgcn_sched_barrier(0); } while (0)
#define LSPIN(cond) do { int sp_ = 0; _Pragma("nounroll") while (cond) { __builtin_amdgcn_s_sleep(1); if (++sp_ > LDS_CAP) break; } __builtin_amdgcn_fence(__ATOMIC_ACQUIRE, "workgroup"); } while (0)
#define STAMP(i) do {} while (0)
#define STAMPC(i) do {} while (0)
#define STAMP_OUT() do {} while (0)
#define STR2(x) #x
#define STR(x) STR2(x)

__global__ void __launch_bounds__(512, 2)
k_fused(const u16* __restrict__ x, const u16* __restrict__ K, const u16* __restrict__ V, const u16* __restrict__ wq, const u16* __restrict__ wo,
        const u16* __restrict__ w1, const u16* __restrict__ b1, const u16* __restrict__ w2, const u16* __restrict__ b2, u16* __restrict__ out,
        unsigned char* __restrict__ ws) {
  extern __shared__ __attribute__((aligned(16))) unsigned char lds[];
  LAS unsigned char* L8 = (LAS unsigned char*)lds;
  volatile LAS unsigned* CTRL = (volatile LAS unsigned*)(L8 + L_CTRL);
  const int tid = threadIdx.x, lane = tid & 63, wave = __builtin_amdgcn_readfirstlane(tid >> 6), b = blockIdx.x;
  const int g = lane >> 4, c = lane & 15;
  const int kvh = XMAP ? (b & 7) : (b >> 5), split = XMAP ? (b >> 3) : (b & 31);
  const int bhead = 4 * (b & 7) + (b >> 6), bcg = (b >> 3) & 7;
  const __amdgpu_buffer_rsrc_t rs = __builtin_amdgcn_make_buffer_rsrc((void*)ws, 0, W_END, 0x00020000);
  {
    const u64 kb = (u64)K + (u64)kvh * (SK * DH * 2) - 3072, vb = (u64)V + (u64)kvh * (SK * DH * 2), pool = (u64)ws + W_POOL + kvh * 256;
    const unsigned vk0 = (4 * (c & 3) + (c >> 2)) * 256 + g * 16 + 3072;
    const unsigned vinit = lane == C_NTOT ? 0x7fffffffu : 0u;
    const u64 wqb = (u64)wq + (u64)((split & 3) * 128 * (HQ * DH) + kvh * 512 + (split >> 2) * 64) * 2;
    const u64 wob = (u64)wo + (u64)(bhead * DH * D + bcg * 64) * 2;
    const unsigned vw0 = (lane >> 3) * (4 * HQ * DH * 2) + (lane & 7) * 16;
    unsigned sq, st, st1, st2, spk, spv, sa, sb, sm0;
    unsigned vt, va, vc;
#define GL(v, off) "global_load_lds_dwordx4 %[" v "], s[90:91] offset:" off " nt\n\t"
#define PUB(reg, off) "v_mov_b32 %[vt], %[" reg "]\n\tds_write_b32 %[vc], %[vt] offset:" off "\n\t"
#define LST(i)
#define LSTQ(qq, i)
    asm volatile(
        "s_cmp_lg_u32 %[wave], 0\n\t"
        "s_cbranch_scc1 Lld_end\n\t"
        "s_mov_b32 %[sq], 0\n\t"
        "s_mov_b32 %[spk], 0\n\t"
        "s_mov_b32 %[spv], 0\n\t"
        "v_mov_b32 %[vc], %[ctrl]\n\t"
        "v_lshrrev_b32 %[vt], 2, %[vv]\n\t"
        "v_add_u32 %[vt], %[vc], %[vt]\n\t"
        "ds_write_b32 %[vt], %[vinit]\n\t"
        LST("12")
        "s_mov_b32 m0, %[lx]\n\t"
        "s_mov_b32 s90, %[wql]\n\t"
        "s_mov_b32 s91, %[wqh]\n\t"
        "global_load_lds_dwordx4 %[vv], %[xp]\n\t"
        "s_mov_b32 m0, %[lwo]\n\t"
        "s_mov_b32 %[sb], 4\n\t"
        "Lld_wq:\n\t"
        GL("vw0", "0") GL("vw1", "1024") GL("vw2", "2048") GL("vw3", "3072")
        "s_addk_i32 m0, 0x1000\n\t"
        "s_add_u32 s90, s90, 0x40000\n\t"
        "s_addc_u32 s91, s91, 0\n\t"
        "s_sub_u32 %[sb], %[sb], 1\n\t"
        "s_cmp_lg_u32 %[sb], 0\n\t"
        "s_cbranch_scc1 Lld_wq\n\t"
        "Lld_loop:\n\t"
        "s_cmp_lt_u32 %[sq], %[nstat]-2\n\t"
        "s_cbranch_scc1 Lld_noclaim\n\t"
        "s_add_u32 %[sa], %[sq], 2\n\t"
        "s_lshl_b64 exec, 1, %[sa]\n\t"
        "v_mov_b32 %[vt], 0\n\t"
        "v_mov_b32 %[va], 1\n\t"
        "global_atomic_add %[va], %[vt], %[va], %[pool] sc0\n\t"
        "s_mov_b64 exec, -1\n\t"
        "Lld_noclaim:\n\t"
        "s_add_u32 %[st], %[tile0], %[sq]\n\t"
        "s_cmp_lt_u32 %[sq], %[nstat]\n\t"
        "s_cbranch_scc1 Lld_havetile\n\t"
        "v_readlane_b32 %[sa], %[va], %[sq]\n\t"
        "s_nop 0\n\t"
        "s_cmp_ge_u32 %[sa], %[npool]\n\t"
        "s_cbranch_scc1 Lld_tail\n\t"
        "s_cmp_ge_u32 %[sq], %[nstat]+%[npool]\n\t"
        "s_cbranch_scc1 Lld_tail\n\t"
        "s_and_b32 %[st], %[sa], 31\n\t"
        "s_lshl_b32 %[st], %[st], 6\n\t"
        "s_lshr_b32 %[sa], %[sa], 5\n\t"
        "s_add_u32 %[st], %[st], %[sa]\n\t"
        "s_add_u32 %[st], %[st], %[nstat]\n\t"
        "Lld_havetile:\n\t"
        "s_cmp_lt_u32 %[sq], 8\n\t"
        "s_cbranch_scc1 Lld_free\n\t"
        "s_and_b32 %[sa], %[sq], 7\n\t"
        "s_lshl_b32 %[sa], %[sa], 2\n\t"
        "s_mov_b32 %[sb], %[cap]\n\t"
        "Lld_spin:\n\t"
        "v_add_u32 %[vt], %[sa], %[vc]\n\t"
        "ds_read_b32 %[vt], %[vt] offset:64\n\t"
        "s_waitcnt lgkmcnt(0)\n\t"
        "v_readfirstlane_b32 %[sm0], %[vt]\n\t"
        "s_nop 0\n\t"
        "s_add_u32 %[sm0], %[sm0], 7\n\t"
        "s_cmp_ge_u32 %[sm0], %[sq]\n\t"
        "s_cbranch_scc1 Lld_free\n\t"
        "s_waitcnt vmcnt(0)\n\t"
        "s_mov_b32 %[spk], %[sq]\n\t"
        "s_sub_u32 %[spv], %[sq], 2\n\t"
        PUB("spk", "0") PUB("spv", "4")
        "s_sleep 1\n\t"
        "s_sub_u32 %[sb], %[sb], 1\n\t"
        "s_cmp_lg_u32 %[sb], 0\n\t"
        "s_cbranch_scc1 Lld_spin\n\t"
        "Lld_free:\n\t"
        "s_lshl_b32 %[sa], %[st], 13\n\t"
        "s_add_u32 s90, %[kbl], %[sa]\n\t"
        "s_addc_u32 s91, %[kbh], 0\n\t"
        "s_and_b32 %[sa], %[sq], 7\n\t"
        "s_lshl_b32 m0, %[sa], 14\n\t"
        "s_nop 0\n\t"
        GL("vk0", "0") GL("vk1", "1024") GL("vk2", "2048") GL("vk3", "3072")
        MIDW
        "s_addk_i32 m0, 0x1000\n\t"
        "s_add_u32 s90, s90, 0x1000\n\t"
        "s_addc_u32 s91, s91, 0\n\t"
        GL("vk0", "0") GL("vk1", "1024") GL("vk2", "2048") GL("vk3", "3072")
        "s_cmp_lt_u32 %[sq], 2\n\t"
        "s_cbranch_scc1 Lld_next\n\t"
        "s_waitcnt vmcnt(" STR(INFL) ")\n\t"
        "s_cmp_lg_u32 %[sq], 2\n\t"
        "s_cbranch_scc1 Lld_nobar\n\t"
        "s_waitcnt vmcnt(24)\n\t"
        LST("13")
        "s_waitcnt lgkmcnt(0)\n\t"
        "s_barrier\n\t"
        "Lld_nobar:\n\t"
        "s_cmp_lt_u32 %[sq], 4\n\t"
        "s_cbranch_scc1 Lld_nopk\n\t"
        "s_sub_i32 %[sa], %[sq], 1\n\t"
        "s_max_i32 %[spk], %[spk], %[sa]\n\t"
        PUB("spk", "0")
        "Lld_nopk:\n\t"
        "s_lshl_b32 %[sa], %[st2], 13\n\t"
        "s_add_u32 s90, %[vbl], %[sa]\n\t"
        "s_addc_u32 s91, %[vbh], 0\n\t"
        "s_sub_u32 %[sa], %[sq], 2\n\t"
        "s_and_b32 %[sa], %[sa], 7\n\t"
        "s_lshl_b32 %[sa], %[sa], 14\n\t"
        "s_add_u32 m0, %[sa], 0x2000\n\t"
        "s_nop 0\n\t"
        GL("vv", "0") GL("vv", "1024") GL("vv", "2048") GL("vv", "3072")
        MIDW
        "s_addk_i32 m0, 0x1000\n\t"
        "s_add_u32 s90, s90, 0x1000\n\t"
        "s_addc_u32 s91, s91, 0\n\t"
        GL("vv", "0") GL("vv", "1024") GL("vv", "2048") GL("vv", "3072")
        "s_cmp_eq_u32 %[sq], 2\n\t"
        "s_cbranch_scc1 Lld_next\n\t"
        "s_waitcnt vmcnt(" STR(INFL) ")\n\t"
        "s_sub_i32 %[sa], %[sq], 3\n\t"
        "s_max_i32 %[spv], %[spv], %[sa]\n\t"
        PUB("spv", "4")
        LSTQ("4", "2") LSTQ("8", "3") LSTQ("16", "4") LSTQ("32", "10") LSTQ("48", "11")
        "Lld_next:\n\t"
        "s_mov_b32 %[st2], %[st1]\n\t"
        "s_mov_b32 %[st1], %[st]\n\t"
        "s_add_u32 %[sq], %[sq], 1\n\t"
        "s_branch Lld_loop\n\t"
        "Lld_tail:\n\t"
        PUB("sq", "8")
        "s_mov_b32 %[sb], 2\n\t"
        "Lld_tail2:\n\t"
        "s_lshl_b32 %[sa], %[st2], 13\n\t"
        "s_add_u32 s90, %[vbl], %[sa]\n\t"
        "s_addc_u32 s91, %[vbh], 0\n\t"
        "s_sub_u32 %[sa], %[sq], %[sb]\n\t"
        "s_and_b32 %[sa], %[sa], 7\n\t"
        "s_lshl_b32 %[sa], %[sa], 14\n\t"
        "s_add_u32 m0, %[sa], 0x2000\n\t"
        "s_nop 0\n\t"
        GL("vv", "0") GL("vv", "1024") GL("vv", "2048") GL("vv", "3072")
        MIDW
        "s_addk_i32 m0, 0x1000\n\t"
        "s_add_u32 s90, s90, 0x1000\n\t"
        "s_addc_u32 s91, s91, 0\n\t"
        GL("vv", "0") GL("vv", "1024") GL("vv", "2048") GL("vv", "3072")
        "s_mov_b32 %[st2], %[st1]\n\t"
        "s_sub_u32 %[sb], %[sb], 1\n\t"
        "s_cmp_lg_u32 %[sb], 0\n\t"
        "s_cbranch_scc1 Lld_tail2\n\t"
        "s_waitcnt vmcnt(40)\n\t"
        "s_sub_i32 %[sa], %[sq], 1\n\t"
        PUB("sa", "0")
        "s_waitcnt vmcnt(32)\n\t"
        "s_sub_i32 %[sa], %[sq], 3\n\t"
        "s_max_i32 %[spv], %[spv], %[sa]\n\t"
        PUB("spv", "4")
        "v_add_u32 %[vw1], 0x1c00, %[vo0]\n\t"
        "v_add_u32 %[vw2], 0x3800, %[vo0]\n\t"
        "v_add_u32 %[vw3], 0x5400, %[vo0]\n\t"
        "s_mov_b32 s90, %[wol]\n\t"
        "s_mov_b32 s91, %[woh]\n\t"
        "s_mov_b32 m0, %[lwo]\n\t"
        "s_waitcnt vmcnt(24)\n\t"
        PUB("sq", "0")
        LST("5")
#define WO4 GL("vo0", "0") GL("vw1", "1024") GL("vw2", "2048") GL("vw3", "3072") "s_addk_i32 m0, 0x1000\n\ts_add_u32 s90, s90, 0x8000\n\ts_addc_u32 s91, s91, 0\n\t"
        WO4 WO4
        "s_sub_u32 %[spv], %[sq], 2\n\t"
        "s_waitcnt vmcnt(24)\n\t"
        PUB("spv", "4")
        WO4 WO4
        "s_add_u32 %[spv], %[spv], 1\n\t"
        "s_waitcnt vmcnt(24)\n\t"
        PUB("spv", "4")
        "s_waitcnt vmcnt(16)\n\t"
        PUB("sq", "4")
        LST("1")
        "s_waitcnt lgkmcnt(0)\n\t"
        "s_barrier\n\t"
        "s_waitcnt vmcnt(0)\n\t"
        PUB("sq", "28")
        LST("6")
        "s_waitcnt lgkmcnt(0)\n\t"
        "s_endpgm\n\t"
        "Lld_end:"
        : [sq] "=&s"(sq), [st] "=&s"(st), [st1] "=&s"(st1), [st2] "=&s"(st2), [spk] "=&s"(spk), [spv] "=&s"(spv), [sa] "=&s"(sa), [sb] "=&s"(sb), [sm0] "=&s"(sm0),
          [vt] "=&v"(vt), [va] "=&v"(va), [vc] "=&v"(vc)
        : [wave] "s"(wave), [kbl] "s"((unsigned)kb), [kbh] "s"((unsigned)(kb >> 32)), [vbl] "s"((unsigned)vb), [vbh] "s"((unsigned)(vb >> 32)), [pool] "s"(pool), [tile0] "s"(split * (POOLC ? NSTAT : NT)), [vk0] "v"(vk0), [vk1] "v"(vk0 + 64 - 1024), [vk2] "v"(vk0 + 128 - 2048),
          [vk3] "v"(vk0 + 192 - 3072), [vv] "v"(lane * 16), [vinit] "v"(vinit), [ctrl] "i"(L_CTRL), [nstat] "i"(NSTAT), [npool] "i"(NPOOL), [cap] "i"(LDS_CAP), [st0] "i"(L_ST - L_CTRL),
          [xp] "s"(x), [wql] "s"((unsigned)wqb), [wqh] "s"((unsigned)(wqb >> 32)), [vw0] "v"(vw0), [vw1] "v"(vw0 + 8192 - 1024), [vw2] "v"(vw0 + 16384 - 2048),
          [vw3] "v"(vw0 + 24576 - 3072), [lx] "i"(L_MRG), [lwo] "i"(L_WO), [wol] "s"((unsigned)wob), [woh] "s"((unsigned)(wob >> 32)), [vo0] "v"((lane >> 3) * (D * 2) + (lane & 7) * 16), "s"(w1), "s"(b1), "s"(w2), "s"(b2), "s"(out)
        : "memory", "scc", "s90", "s91", "s92", "s93");
  }
  if (wave <= 4) {
    BAR();
    const u32x4 xv = *(const LAS u32x4*)(L8 + L_MRG + lane * 16);
    const u32x2 xo = *(const LAS u32x2*)(L8 + L_MRG + ((split & 3) * 128 + 32 * (wave - 1) + 4 * (lane >> 3)) * 2);
    u32x4 wqv[4];
#pragma unroll
    for (int i = 0; i < 4; ++i) wqv[i] = *(const LAS u32x4*)(L8 + L_WO + (wave - 1) * 4096 + i * 1024 + lane * 16);
    constexpr unsigned ONES = 0x3f803f80u;
    float S, Q;
    wave_sum2(dot2(xv[0], ONES, dot2(xv[1], ONES, 0.f)) + dot2(xv[2], ONES, dot2(xv[3], ONES, 0.f)),
              dot2(xv[0], xv[0], dot2(xv[1], xv[1], 0.f)) + dot2(xv[2], xv[2], dot2(xv[3], xv[3], 0.f)), S, Q);
    const float md = S * (1.0f / D);
    const float mb = rbf(md), rden = ln_rden(fmaxf(fmaf(-md, md, Q * (1.0f / D)), 0.f));
    const f32x2 xa = ln_apply2(unpk(xo[0]), mb, rden), xb = ln_apply2(unpk(xo[1]), mb, rden);
    STAMP(10);
    f32x2 a2[4];
#pragma unroll
    for (int j = 0; j < 4; ++j) a2[j] = (unpk(wqv[0][j]) * xa[0] + unpk(wqv[1][j]) * xa[1]) + (unpk(wqv[2][j]) * xb[0] + unpk(wqv[3][j]) * xb[1]);
    float a[8];
#pragma unroll
    for (int j = 0; j < 8; ++j) a[j] = a2[j >> 1][j & 1];
#pragma unroll
    for (int j = 0; j < 4; ++j) a[j] = swap32_add(a[j], a[j + 4]);
#pragma unroll
    for (int j = 0; j < 2; ++j) a[j] = swap16_add(a[j], a[j + 2]);
    { const float keep = (lane & 8) ? a[1] : a[0], send = (lane & 8) ? a[0] : a[1]; a[0] = keep + dpp<XOR8>(send); }
    ((LAS float*)(L8 + L_QP))[(wave - 1) * 64 + 8 * (lane & 7) + (lane >> 3)] = a[0];
    LDS_WAIT();
    if (lane == 0) __hip_atomic_fetch_add((LAS unsigned*)(L8 + L_CTRL) + C_QCNT, 1u, __ATOMIC_RELAXED, __HIP_MEMORY_SCOPE_WORKGROUP);
    STAMP(1);
  } else if (wave == HELPER) {
    const u64 epoch = __hip_atomic_load((GAS u64*)(ws + W_EPOCH), __ATOMIC_RELAXED, __HIP_MEMORY_SCOPE_AGENT);
    const u32x4 sv = {SENT, SENT, SENT, SENT};
    if (lane < 40 && !(EXP & 4)) {
#pragma unroll
      for (int h = 0; h < 4; ++h) __builtin_amdgcn_raw_buffer_store_b128(sv, rs, W_PA + (((4 * kvh + h) * 5 + (lane >> 3)) * NSPLIT + split) * 128 + (lane & 7) * 16, 0, AUX_SC1);
    }
    if (lane < 24) __builtin_amdgcn_raw_buffer_store_b128(sv, rs, lane < 16 ? W_Y + (bhead * D + bcg * 64) * 4 + lane * 16 : W_Y16 + (bhead * D + bcg * 64) * 2 + (lane - 16) * 16, 0, AUX_SC1);
    if (split == 0 && lane == 0) __builtin_amdgcn_raw_buffer_store_b32(0u, rs, W_POOL + kvh * 256, 0, AUX_SC1);
    BAR();
    asm volatile("s_waitcnt vmcnt(0)" ::: "memory");
    STAMP(10);
    const u64 tag = (epoch + 1) << 24;
    const unsigned tlo = (unsigned)tag, thi = (unsigned)(tag >> 32);
    LSPIN(CTRL[C_QCNT] < 4u);
    const LAS float* qp = (const LAS float*)(L8 + L_QP);
    const unsigned qs = __float_as_uint((qp[lane] + qp[64 + lane]) + (qp[128 + lane] + qp[192 + lane]));
    __hip_atomic_store((GAS u64*)(ws + W_QG) + (kvh * 4 + (split & 3)) * 512 + (split >> 2) * 64 + lane, tag | (u64)((qs + 0x80u) >> 8), __ATOMIC_RELAXED, __HIP_MEMORY_SCOPE_AGENT);
    if (lane == 0) __hip_atomic_store((GAS u64*)(ws + W_FLAG) + b, epoch + 1, __ATOMIC_RELAXED, __HIP_MEMORY_SCOPE_AGENT);
    STAMP(1);
    { const u64 tp = rt(); _Pragma("nounroll") while (rt() - tp < QD) __builtin_amdgcn_s_sleep(2); }
    STAMP(12);
    {
      u32x4 gq[16];
      _Pragma("nounroll") for (int sp = 0; sp < SPIN_CAP; ++sp) { CFENCE();
#pragma unroll
        for (int k = 0; k < 16; ++k) gq[k] = (EXP & 1) ? u32x4{tlo, thi, tlo, thi} : __builtin_amdgcn_raw_buffer_load_b128(rs, W_QG + kvh * 16384 + k * 1024 + lane * 16, 0, AUX_SC1);
        unsigned bad = 0;
#pragma unroll
        for (int k = 0; k < 16; ++k) bad |= (((gq[k][0] ^ tlo) | (gq[k][2] ^ tlo)) & 0xff000000u) | (gq[k][1] ^ thi) | (gq[k][3] ^ thi);
        if (__all(bad == 0u)) break;
        __builtin_amdgcn_s_sleep(QSLEEP);
      }
#pragma unroll
      for (int k = 0; k < 4; ++k) {
        const float lo = (__uint_as_float(gq[k][0] << 8) + __uint_as_float(gq[4 + k][0] << 8)) + (__uint_as_float(gq[8 + k][0] << 8) + __uint_as_float(gq[12 + k][0] << 8));
        const float hi = (__uint_as_float(gq[k][2] << 8) + __uint_as_float(gq[4 + k][2] << 8)) + (__uint_as_float(gq[8 + k][2] << 8) + __uint_as_float(gq[12 + k][2] << 8));
        *(LAS unsigned*)(L8 + L_QS + k * 256 + lane * 4) = pk_bf16(lo, hi);
      }
      LDS_WAIT();
      CTRL[C_QREADY] = 1u;
    }
    STAMP(2);
    LSPIN(CTRL[C_KL] < (unsigned)(NT / 2));
    _Pragma("nounroll") for (int sp = 0; sp < SPIN_CAP; ++sp) { CFENCE();
      const u32x4 t0 = __builtin_amdgcn_raw_buffer_load_b128(rs, W_FLAG + lane * 16, 0, AUX_SC1), t1 = __builtin_amdgcn_raw_buffer_load_b128(rs, W_FLAG + 1024 + lane * 16, 0, AUX_SC1);
      const unsigned el = (unsigned)(epoch + 1), eh = (unsigned)((epoch + 1) >> 32);
      const unsigned bad = (t0[0] ^ el) | (t0[2] ^ el) | (t1[0] ^ el) | (t1[2] ^ el) | (t0[1] ^ eh) | (t0[3] ^ eh) | (t1[1] ^ eh) | (t1[3] ^ eh);
      if (__all(bad == 0u)) break;
      __builtin_amdgcn_s_sleep(8);
    }
    if (b == 0 && lane == 0) __hip_atomic_store((GAS u64*)(ws + W_EPOCH), epoch + 1, __ATOMIC_RELAXED, __HIP_MEMORY_SCOPE_AGENT);
    asm volatile("s_waitcnt vmcnt(0)" ::: "memory");
    STAMP(11);
  } else {
    BAR();
  }

  const int tw = wave & 3;
  unsigned pr[12], xr = 0, pbb = 0;
  if (wave >= 4 && !(EXP & 32)) {
    const int t = tid - 256;
#pragma unroll
    for (int i = 0; i < 3; ++i) {
      const u16* pp = i == 0 ? w1 : i == 1 ? b1 : w2;
      const u32x2 f4 = *(const GAS u32x2*)(pp + 4 * t);
      pr[4 * i] = f4[0]; pr[4 * i + 1] = f4[1]; pr[4 * i + 2] = *(const GAS unsigned*)(pp + 1024 + 2 * t); pr[4 * i + 3] = pp[1536 + t];
    }
    xr = *(const GAS unsigned*)(x + 128 * tw + 8 * (lane & 15) + 2 * (lane >> 4));
    pbb = *(const GAS u16*)b2;
  } else {
#pragma unroll
    for (int i = 0; i < 12; ++i) pr[i] = 0;
    xr = *(const GAS unsigned*)(x + 2 * b);
  }
  if (wave != HELPER) {
    f32x4 o[8];
#pragma unroll
    for (int d = 0; d < 8; ++d) o[d] = f32x4{0.f, 0.f, 0.f, 0.f};
    float m = -INFINITY, l = 0.f;
    u32x4 qf[4];
#pragma unroll
    for (int s = 0; s < 4; ++s) qf[s] = u32x4{0u, 0u, 0u, 0u};
    constexpr float SCALE = 0.08838834764831845f;
#define TICKET() __builtin_amdgcn_readfirstlane(lane == 0 ? __hip_atomic_fetch_add((LAS unsigned*)(L8 + L_CTRL) + C_TICKET, 1u, __ATOMIC_RELAXED, __HIP_MEMORY_SCOPE_WORKGROUP) : 0u)
    const LAS unsigned char* ring = L8 + L_RING + lane * 16;
    unsigned q = TICKET();
    for (int it = 0;; ++it) {
      u32x4 tk[8], tv[8];
      const int s = q & (NSLOT - 1);
      const LAS unsigned char* sp = ring + s * SLOT;
      unsigned ntot;
#define WAIT_LANDED(idx) do { int sp_ = 0; _Pragma("nounroll") for (;;) { const u32x4 cw_ = *(const volatile LAS u32x4*)(L8 + L_CTRL); \
        ntot = __builtin_amdgcn_readfirstlane(cw_[C_NTOT]); \
        if (__builtin_amdgcn_readfirstlane(cw_[idx]) > q || ntot <= q || ++sp_ > LDS_CAP) break; __builtin_amdgcn_s_sleep(1); } \
        __builtin_amdgcn_fence(__ATOMIC_ACQUIRE, "workgroup"); } while (0)
#define READ_V() do { WAIT_LANDED(C_VL); \
      _Pragma("unroll") for (int j = 0; j < 8; ++j) tv[j] = *(const LAS u32x4*)(sp + 8192 + j * 1024); \
      LDS_WAIT(); CTRL[C_FREE + s] = q + 1; \
      asm volatile("" : "+v"(tv[0]), "+v"(tv[1]), "+v"(tv[2]), "+v"(tv[3]), "+v"(tv[4]), "+v"(tv[5]), "+v"(tv[6]), "+v"(tv[7]) :: "memory"); } while (0)
      WAIT_LANDED(C_KL);
      if (ntot <= q || q >= (unsigned)(NT + NPOOL)) break;
      if (wave < 4) STAMP(5);
#pragma unroll
      for (int j = 0; j < 8; ++j) tk[j] = *(const LAS u32x4*)(sp + j * 1024);
      if (it == 0) {
        READ_V();
        LSPIN(CTRL[C_QREADY] == 0u);
        if (c < 4) {
#pragma unroll
          for (int s2 = 0; s2 < 4; ++s2) qf[s2] = *(const LAS u32x4*)(L8 + L_QS + c * 256 + s2 * 64 + g * 16);
        }
        STAMP(3);
      }
      f32x4 s0 = {0.f, 0.f, 0.f, 0.f}, s1 = {0.f, 0.f, 0.f, 0.f};
#pragma unroll
      for (int k = 0; k < 4; ++k) { s0 = mfma16(tk[k], qf[k], s0); s1 = mfma16(tk[4 + k], qf[k], s1); }
      unsigned qn = 0x7fffffffu;
      if (wave < 4 || q < (unsigned)(NSTAT - RETIRE)) qn = TICKET();
      f32x2 sc[4];
      sc[0] = rbf2(rbf2(f32x2{s0[0], s0[1]}) * SCALE); sc[1] = rbf2(rbf2(f32x2{s0[2], s0[3]}) * SCALE);
      sc[2] = rbf2(rbf2(f32x2{s1[0], s1[1]}) * SCALE); sc[3] = rbf2(rbf2(f32x2{s1[2], s1[3]}) * SCALE);
#pragma unroll
      for (int r = 0; r < 4; ++r) sc[r] *= LOG2E;
      const float mx = xg_max2(vmax3(vmax3(sc[0][0], sc[0][1], sc[1][0]), vmax3(sc[1][1], sc[2][0], sc[2][1]), vmax(sc[3][0], sc[3][1])));
      if (__any(mx > m)) {
        const float mn = vmax(m, __builtin_ceilf(mx));
        const float alpha = __builtin_amdgcn_exp2f(m - mn);
        m = mn; l *= alpha;
#pragma unroll
        for (int d = 0; d < 8; ++d) o[d] *= alpha;
      }
      float p[8];
#pragma unroll
      for (int r = 0; r < 8; ++r) p[r] = __builtin_amdgcn_exp2f(sc[r >> 1][r & 1] - m);
      l += xg_sum(((p[0] + p[1]) + (p[2] + p[3])) + ((p[4] + p[5]) + (p[6] + p[7])));
      u32x4 pf = {pk_bf16(p[0], p[1]), pk_bf16(p[2], p[3]), pk_bf16(p[4], p[5]), pk_bf16(p[6], p[7])};
      asm volatile("" : "+v"(pf), "+v"(l));
      if (wave < 4) STAMP(6);
      if (it != 0) {
        WAIT_LANDED(C_VL);
#pragma unroll
        for (int j = 0; j < 8; ++j) tv[j] = *(const LAS u32x4*)(sp + 8192 + j * 1024);
      }
      if (wave < 4) STAMP(7);
#pragma unroll
      for (int d = 0; d < 8; ++d) {
        u32x4 a;
#pragma unroll
        for (int jj = 0; jj < 4; ++jj) a[jj] = __builtin_amdgcn_perm(tv[2 * jj + 1][d >> 1], tv[2 * jj][d >> 1], (d & 1) ? 0x07060302u : 0x05040100u);
        o[d] = mfma16(a, pf, o[d]);
      }
      if (qn >= ntot && wave < 4) break;
      if (it != 0) { LDS_WAIT(); CTRL[C_FREE + s] = q + 1; }
      if (qn >= ntot) break;
      q = qn;
    }
    if (c < 4) {
      const int ci = wave > HELPER ? wave - 2 : wave - 1;
      LAS float* rec = (LAS float*)(L8 + L_MRG) + (ci * 4 + c) * 132;
      if (g == 0) *(LAS f32x2*)(L8 + L_MML + c * 64 + ci * 8) = f32x2{m, l};
#pragma unroll
      for (int d = 0; d < 8; ++d) *(LAS f32x4*)(rec + d * 16 + g * 4) = o[d];
    }
    STAMP(4);
  }
  if (wave < 4) {
    BAR();
    if (wave != 1) return;
    const int o_own = W_Y + ((lane & 31) * D + 2 * b + (lane >> 5)) * 4;
    BAR();
    { const u64 tp = rt(); _Pragma("nounroll") while (rt() - tp < CD + 8) {} }
    unsigned yo = 0;
    _Pragma("nounroll") for (int sp = 0; sp < SPIN_CAP; ++sp) { CFENCE();
      yo = __builtin_amdgcn_raw_buffer_load_b32(rs, o_own, 0, AUX_SC1);
      if (!__any(yo == SENT)) break;
      __builtin_amdgcn_s_sleep(1);
    }
    float v = __uint_as_float(yo);
    v += dpp<XOR1>(v); v += dpp<XOR2>(v); v += dpp<XOR7>(v); v += dpp<XOR8>(v);
    v = swap16_add(v, v);
    *(LAS float*)(L8 + L_STAT + 64 + (lane >> 5) * 4) = rbf(((lane >> 5) ? bfhi(xr) : bf2f(xr)) + rbf(v));
    BAR();
    return;
  }

  float w1f[7], b1f[7], w2f[7];
#pragma unroll
  for (int i = 0; i < 7; ++i) { w1f[i] = unpk(pr[i >> 1])[i & 1] * GA; b1f[i] = unpk(pr[4 + (i >> 1)])[i & 1] * GA; w2f[i] = unpk(pr[8 + (i >> 1)])[i & 1] * (1.0f / GA); asm volatile("" : "+v"(w1f[i]), "+v"(b1f[i]), "+v"(w2f[i])); }
  const int hb = lane >> 5;
  int a_rec = L_MRG + (hb * 12 + tw) * 528 + (lane & 31) * 16;
  int a_mml = L_MML + tw * 64 + hb * 24;
  int o_pa = W_PA + (((4 * kvh + tw) * 5 + (lane >> 3)) * NSPLIT + split) * 128 + (lane & 7) * 16;
  int o_bd = W_PA + (bhead * 5 + tw) * 4096 + lane * 16, o_bm = W_PA + (bhead * 5 + 4) * 4096 + lane * 16;
  int o_y = W_Y + (bhead * D + bcg * 64 + 4 * c) * 4;
  int o_c = W_Y16 + (g * D + 128 * tw + 8 * c) * 2;
  int o_y16 = W_Y16 + (bhead * D + bcg * 64 + 4 * c) * 2;
  int a_st = tw * 16 + g * 4;
  int a_ob = L_OB + (tw * 32 + 4 * (lane & 7) + (lane >> 4)) * 2;
  f32x2 xf = unpk(xr); float bbf = bf2f(pbb);
  unsigned sel_lo = 0x00003f80u, sel_hi = 0x3f800000u;
  asm volatile("" : "+v"(sel_lo), "+v"(sel_hi));
  asm volatile("" : "+v"(a_rec), "+v"(a_mml), "+v"(o_pa), "+v"(o_bd), "+v"(o_bm), "+v"(o_y), "+v"(o_c), "+v"(o_y16), "+v"(a_st), "+v"(a_ob), "+v"(xf), "+v"(bbf));
  BAR();
  {
    const f32x2 m0 = *(const LAS f32x2*)(L8 + a_mml), m1 = *(const LAS f32x2*)(L8 + a_mml + 8), m2 = *(const LAS f32x2*)(L8 + a_mml + 16);
    const f32x4 r0 = *(const LAS f32x4*)(L8 + a_rec), r1 = *(const LAS f32x4*)(L8 + a_rec + 2112), r2 = *(const LAS f32x4*)(L8 + a_rec + 4224);
    float M = vmax3(m0[0], m1[0], m2[0]), t;
    asm("v_mov_b32 %1, %0\n\ts_nop 1\n\tv_permlane32_swap_b32 %0, %1\n\tv_max_f32 %0, %0, %1" : "+v"(M), "=&v"(t));
    const float e0 = __builtin_amdgcn_exp2f(m0[0] - M), e1 = __builtin_amdgcn_exp2f(m1[0] - M), e2 = __builtin_amdgcn_exp2f(m2[0] - M);
    const float Lp = e0 * m0[1] + e1 * m1[1] + e2 * m2[1];
    const f32x4 acc = r0 * e0 + r1 * e1 + r2 * e2;
    u32x4 ub;
    ub[0] = __float_as_uint(swap32_add(acc[0], 0.5f * M));
    ub[1] = __float_as_uint(swap32_add(acc[1], Lp));
    ub[2] = __float_as_uint(swap32_add(acc[2], Lp));
    ub[3] = __float_as_uint(swap32_add(acc[3], Lp));
#pragma unroll
    for (int k = 0; k < 4; ++k) ub[k] = min(ub[k], SENT - 1u);
    if (lane < 40) __builtin_amdgcn_raw_buffer_store_b128(ub, rs, o_pa, 0, AUX_SC1);
  }
  STAMP(5);
  LSPIN(CTRL[C_WO] == 0u);
  u32x4 wof[4];
#pragma unroll
  for (int s = 0; s < 4; ++s) {
    const int a = L_WO + (64 * (g & 1) + 8 * (c >> 2) + 2 * tw + (g >> 1)) * 128 + s * 32 + (c & 3) * 8;
    const u32x2 lo = __builtin_bit_cast(u32x2, __builtin_amdgcn_ds_read_tr16_b64_v4i16((LAS s16x4*)(L8 + a)));
    const u32x2 hi = __builtin_bit_cast(u32x2, __builtin_amdgcn_ds_read_tr16_b64_v4i16((LAS s16x4*)(L8 + a + 32 * 128)));
    wof[s] = u32x4{lo[0], lo[1], hi[0], hi[1]};
  }
  if (BD) { const u64 tp = rt(); _Pragma("nounroll") while (rt() - tp < BD) {} }
  {
    u32x4 a[4]; u32x2 mlv[4];
    {
      u64 k0, k1, k2, k3; unsigned cnt, t0, t1;
#define B_LD(k, off) "s_mov_b64 exec, %[k" k "]\n\tbuffer_load_dwordx2 %[m" k "], %[om], %[rs], 0 offen offset:" off " sc1\n\tbuffer_load_dwordx4 %[a" k "], %[od], %[rs], 0 offen offset:" off " sc1\n\t"
      asm volatile(
          "s_mov_b64 %[k0], -1\n\ts_mov_b64 %[k1], -1\n\ts_mov_b64 %[k2], -1\n\ts_mov_b64 %[k3], -1\n\ts_mov_b32 %[cnt], %[cap]\n\t"
          "Lb_poll:\n\t"
          B_LD("0", "0") B_LD("1", "1024") B_LD("2", "2048") B_LD("3", "3072")
          "s_mov_b64 exec, -1\n\t"
          "s_waitcnt vmcnt(6)\n\t"
          "v_max3_u32 %[t0], v100, v101, v102\n\tv_max3_u32 %[t0], %[t0], v103, v116\n\tv_max_u32 %[t0], %[t0], v117\n\t"
          "s_waitcnt vmcnt(4)\n\t"
          "v_max3_u32 %[t1], v104, v105, v106\n\tv_cmp_eq_u32 %[k0], -1, %[t0]\n\tv_max3_u32 %[t1], %[t1], v107, v118\n\tv_max_u32 %[t1], %[t1], v119\n\t"
          "s_waitcnt vmcnt(2)\n\t"
          "v_max3_u32 %[t0], v108, v109, v110\n\tv_cmp_eq_u32 %[k1], -1, %[t1]\n\tv_max3_u32 %[t0], %[t0], v111, v120\n\tv_max_u32 %[t0], %[t0], v121\n\t"
          "s_waitcnt vmcnt(0)\n\t"
          "v_max3_u32 %[t1], v112, v113, v114\n\tv_cmp_eq_u32 %[k2], -1, %[t0]\n\tv_max3_u32 %[t1], %[t1], v115, v122\n\tv_max_u32 %[t1], %[t1], v123\n\t"
          "s_or_b64 vcc, %[k0], %[k1]\n\t"
          "v_cmp_eq_u32 %[k3], -1, %[t1]\n\t"
          "s_or_b64 vcc, vcc, %[k2]\n\t"
          "s_sub_u32 %[cnt], %[cnt], 1\n\t"
          "s_cbranch_scc1 Lb_done\n\t"
          "s_or_b64 vcc, vcc, %[k3]\n\t"
          "s_cbranch_scc1 Lb_poll\n\t"
          "Lb_done:"
          : [a0] "={v[100:103]}"(a[0]), [a1] "={v[104:107]}"(a[1]), [a2] "={v[108:111]}"(a[2]), [a3] "={v[112:115]}"(a[3]),
            [m0] "={v[116:117]}"(mlv[0]), [m1] "={v[118:119]}"(mlv[1]), [m2] "={v[120:121]}"(mlv[2]), [m3] "={v[122:123]}"(mlv[3]),
            [k0] "=&s"(k0), [k1] "=&s"(k1), [k2] "=&s"(k2), [k3] "=&s"(k3), [cnt] "=&s"(cnt), [t0] "=&v"(t0), [t1] "=&v"(t1)
          : [om] "{v124}"(o_bm), [od] "{v125}"(o_bd), [rs] "s"(rs), [cap] "i"(SPIN_CAP)
          : "memory", "vcc", "scc");
    }
    STAMP(6);
#define MF(k, i) __uint_as_float(mlv[k][i])
    const float M = xg_max2(xor8_max(vmax(vmax3(MF(0, 0), MF(1, 0), MF(2, 0)), MF(3, 0))));
    const float w0 = __builtin_amdgcn_exp2f(MF(0, 0) - M), w1_ = __builtin_amdgcn_exp2f(MF(1, 0) - M), w2_ = __builtin_amdgcn_exp2f(MF(2, 0) - M), w3 = __builtin_amdgcn_exp2f(MF(3, 0) - M);
    float L = (w0 * MF(0, 1) + w1_ * MF(1, 1)) + (w2_ * MF(2, 1) + w3 * MF(3, 1));
    const f32x4 v = (__builtin_bit_cast(f32x4, a[0]) * w0 + __builtin_bit_cast(f32x4, a[1]) * w1_) + (__builtin_bit_cast(f32x4, a[2]) * w2_ + __builtin_bit_cast(f32x4, a[3]) * w3);
    L = swap32_add(L, L);
    const float u0 = swap32_add(v[0], v[2]), u1 = swap32_add(v[1], v[3]);
    L = swap16_add(L, L);
    float u = swap16_add(u0, u1);
    L += dpp<XOR8>(L); u += dpp<XOR8>(u);
    const float ov = u * __builtin_amdgcn_rcpf(L);
    *(LAS u16*)(L8 + a_ob) = (u16)pk_bf16(ov, ov);
    LDS_WAIT();
    const u32x4 of = *(const LAS u32x4*)(L8 + L_OB + tw * 64 + g * 16);
    const f32x4 z = {0.f, 0.f, 0.f, 0.f};
    const f32x4 y0 = mfma16(wof[0], of, z), y1 = mfma16(wof[1], of, z), y2 = mfma16(wof[2], of, z), y3 = mfma16(wof[3], of, z);
    if (c == 0) {
      LAS f32x4* yp = (LAS f32x4*)(L8 + L_YP + tw * 256 + g * 16);
      yp[0] = y0; yp[4] = y1; yp[8] = y2; yp[12] = y3;
    }
  }
  BAR();
  if (tw < 2 && lane < 16) {
    const LAS f32x4* yp = (const LAS f32x4*)(L8 + L_YP + c * 16);
    const f32x4 y = (yp[0] + yp[16]) + (yp[32] + yp[48]);
    if (tw == 0) {
      const u32x2 uh = {min(pk_bf16(y[0], y[1]), SENT - 1u), min(pk_bf16(y[2], y[3]), SENT - 1u)};
      __builtin_amdgcn_raw_buffer_store_b64(uh, rs, o_y16, 0, AUX_SC1);
    } else {
      u32x4 ub = __builtin_bit_cast(u32x4, y);
#pragma unroll
      for (int k = 0; k < 4; ++k) ub[k] = min(ub[k], SENT - 1u);
      __builtin_amdgcn_raw_buffer_store_b128(ub, rs, o_y, 0, AUX_SC1);
    }
  }
  STAMP(7);
  if (CD) { const u64 tp = rt(); _Pragma("nounroll") while (rt() - tp < CD) {} }
  {
    f32x2 at;
    _Pragma("nounroll") for (int sp = 0; sp < SPIN_CAP; ++sp) { CFENCE();
      u32x4 yv[8];
#pragma unroll
      for (int k = 0; k < 8; ++k) yv[k] = __builtin_amdgcn_raw_buffer_load_b128(rs, o_c, k * 4096, AUX_SC1);
      float s[8];
#pragma unroll
      for (int j = 0; j < 8; ++j) s[j] = 0.f;
#pragma unroll
      for (int k = 0; k < 8; ++k) {
#pragma unroll
        for (int j = 0; j < 8; ++j) s[j] = dot2(yv[k][j >> 1], (j & 1) ? sel_hi : sel_lo, s[j]);
        __builtin_amdgcn_sched_barrier(0);
      }
#pragma unroll
      for (int j = 0; j < 4; ++j) s[j] = swap32_add(s[j], s[j + 4]);
      at = f32x2{swap16_add(s[0], s[2]), swap16_add(s[1], s[3])};
      if (!__any(__builtin_isunordered(at[0], at[1]))) break;
      unsigned mx = 0;
#pragma unroll
      for (int k = 0; k < 8; ++k) mx = max(mx, umax4(yv[k]));
      if (!__any(mx == SENT)) break;
    }
    STAMP(8);
    const f32x2 hs = xf + rbf2(at);
    const unsigned hp = pk_bf16(hs[0], hs[1]);
    *(LAS float*)(L8 + L_STAT + a_st) = row_sum2(dot2(hp, 0x3f803f80u, 0.f), dot2(hp, hp, 0.f));
  }
  BAR();
  STAMP(13);
#define Q4(base) ((*(const LAS f32x4*)(L8 + (base)) + *(const LAS f32x4*)(L8 + (base) + 16)) + (*(const LAS f32x4*)(L8 + (base) + 32) + *(const LAS f32x4*)(L8 + (base) + 48)))
  const f32x2 hh = *(const LAS f32x2*)(L8 + L_STAT + 64);
  {
    const f32x4 sq = Q4(L_STAT);
    const float mean = (sq[0] + sq[1]) * (1.0f / D);
    const float var = fmaxf(fmaf(-mean, mean, (sq[2] + sq[3]) * (1.0f / D)), 0.f);
    const float mb = rbf(mean), rden = ln_rden(var);
    const float mb2 = rbf((mean - mb) * rden), rden2 = ln_rden(var * rden * rden);
    const f32x2 tt = ln_apply2(ln_apply2(hh, mb, rden), mb2, rden2);
    f32x2 a0 = {0.f, 0.f}, a1 = {0.f, 0.f};
#pragma unroll
    for (int i = 0; i < 7; ++i) { const f32x2 gl = gelu2v(tt * w1f[i] + b1f[i]) * w2f[i]; if (i & 1) a1 += gl; else a0 += gl; }
    a0 += a1;
    *(LAS float*)(L8 + L_RED + a_st) = row_sum2(a0[0], a0[1]);
  }
  BAR();
  if (wave == HELPER) {
    const f32x4 r4 = Q4(L_RED);
    const f32x2 r = hh + rbf2(rbf2(f32x2{r4[0] + r4[1], r4[2] + r4[3]}) + bbf);
    if (lane == 0) *(GAS unsigned*)(out + 2 * b) = pk_bf16(r[0], r[1]);
  }
  STAMP_OUT();
}

extern "C" void kernel_launch(void* const* d_in, const int* in_sizes, int n_in, void* d_out, int out_size, void* d_ws, size_t ws_size, hipStream_t stream) {
  (void)hipFuncSetAttribute((const void*)k_fused, hipFuncAttributeMaxDynamicSharedMemorySize, L_END);
  hipLaunchKernelGGL(k_fused, dim3(NWG), dim3(512), L_END, stream, (const u16*)d_in[0], (const u16*)d_in[1], (const u16*)d_in[2], (const u16*)d_in[3],
                     (const u16*)d_in[4], (const u16*)d_in[6], (const u16*)d_in[7], (const u16*)d_in[8], (const u16*)d_in[9], (u16*)d_out, (unsigned char*)d_ws);
}
```

```cpp
#include <hip/hip_runtime.h>
#include <hip/hip_bf16.h>
#include <stdint.h>

#define GAS __attribute__((address_space(1)))
#define LAS __attribute__((address_space(3)))
typedef unsigned u32x4 __attribute__((ext_vector_type(4)));
typedef unsigned u32x2 __attribute__((ext_vector_type(2)));
typedef float f32x4 __attribute__((ext_vector_type(4)));
typedef float f32x2 __attribute__((ext_vector_type(2)));
typedef short bf16x8 __attribute__((ext_vector_type(8)));
typedef short s16x4 __attribute__((ext_vector_type(4)));
typedef __bf16 bf16x2 __attribute__((ext_vector_type(2)));
typedef unsigned short u16;
typedef unsigned long long u64;

constexpr int QD = 120;
constexpr int CD = 50;
constexpr int NSTAT = 59;
constexpr int RETIRE = 8;

constexpr int D = 512, HQ = 32;
constexpr int NWG = 256, NSPLIT = 32;
constexpr int NT = 64;
constexpr int NSLOT = 8, SLOT = 16384;
constexpr int NC = 6;
constexpr int HELPER = 5;
constexpr int L_RING = 0;
constexpr int L_WO = NSLOT * SLOT;
constexpr int L_CTRL = L_WO + 16384;
constexpr int C_KL = 0, C_VL = 1, C_NTOT = 2, C_TICKET = 4, C_QCNT = 5, C_QREADY = 6, C_WO = 7, C_FREE = 16;
constexpr int L_QS = L_CTRL + 256;
constexpr int L_QP = L_QS + 1024;
constexpr int L_MRG = L_QP + 1024;
constexpr int L_MML = L_MRG + NC * 4 * 528;
constexpr int NPOOL = NSPLIT * (NT - NSTAT);
constexpr int L_YP = L_QP;
constexpr int L_STAT = L_MML + 256;
constexpr int L_RED = L_STAT + 80;
constexpr int L_END = L_RED + 64;
static_assert(L_END <= 160 * 1024, "LDS");
constexpr int PREC = 640;
constexpr int W_EPOCH = 0, W_POOL = 4096, W_FLAG = 8192, W_Y16 = 32768, W_Y = 65536, W_QG = 131072, W_PA = 262144, W_END = W_PA + HQ * NSPLIT * PREC;
constexpr unsigned SENT = 0xFFFFFFFFu;
constexpr int AUX_SC1 = 16;
constexpr float LOG2E = 1.4426950408889634f;
constexpr int SPIN_CAP = 1 << 15;
constexpr int LDS_CAP = 1 << 20;

__device__ __forceinline__ float bf2f(unsigned b) { return __builtin_bit_cast(float, b << 16); }
__device__ __forceinline__ float bfhi(unsigned b) { return __builtin_bit_cast(float, b & 0xffff0000u); }
__device__ __forceinline__ f32x2 unpk(unsigned u) { return f32x2{bf2f(u), bfhi(u)}; }
__device__ __forceinline__ unsigned pk_bf16(float lo, float hi) {
  f32x2 v = {lo, hi};
  return __builtin_bit_cast(unsigned, __builtin_convertvector(v, bf16x2));
}
__device__ __forceinline__ float rbf(float x) { return __uint_as_float(pk_bf16(0.0f, x)); }
__device__ __forceinline__ f32x2 rbf2(f32x2 v) { return f32x2{rbf(v[0]), rbf(v[1])}; }
__device__ __forceinline__ float dot2(unsigned a, unsigned b, float c) {
  return __builtin_amdgcn_fdot2_f32_bf16(__builtin_bit_cast(bf16x2, a), __builtin_bit_cast(bf16x2, b), c, false);
}
__device__ __forceinline__ f32x4 mfma16(u32x4 a, u32x4 b, f32x4 c) {
  return __builtin_amdgcn_mfma_f32_16x16x32_bf16(__builtin_bit_cast(bf16x8, a), __builtin_bit_cast(bf16x8, b), c, 0, 0, 0);
}
template <int CTRL> __device__ __forceinline__ float dpp(float x) {
  return __builtin_bit_cast(float, __builtin_amdgcn_mov_dpp(__builtin_bit_cast(int, x), CTRL, 0xf, 0xf, true));
}
constexpr int XOR1 = 0xB1, XOR2 = 0x4E, XOR7 = 0x141, XOR8 = 0x128;
__device__ __forceinline__ float swap16_add(float a, float b) {
  auto s = __builtin_amdgcn_permlane16_swap(__float_as_uint(a), __float_as_uint(b), false, false);
  return __uint_as_float(s[0]) + __uint_as_float(s[1]);
}
__device__ __forceinline__ float swap32_add(float a, float b) {
  auto s = __builtin_amdgcn_permlane32_swap(__float_as_uint(a), __float_as_uint(b), false, false);
  return __uint_as_float(s[0]) + __uint_as_float(s[1]);
}
__device__ __forceinline__ float vmax(float a, float b) { float r; asm("v_max_f32 %0, %1, %2" : "=v"(r) : "v"(a), "v"(b)); return r; }
__device__ __forceinline__ float vmax3(float a, float b, float c) { float r; asm("v_max3_f32 %0, %1, %2, %3" : "=v"(r) : "v"(a), "v"(b), "v"(c)); return r; }
__device__ __forceinline__ float xg_max2(float x) {
  float t;
  asm("v_mov_b32 %1, %0\n\ts_nop 1\n\tv_permlane16_swap_b32 %0, %1\n\tv_max_f32 %0, %0, %1\n\tv_mov_b32 %1, %0\n\ts_nop 1\n\tv_permlane32_swap_b32 %0, %1\n\tv_max_f32 %0, %0, %1"
      : "+v"(x), "=&v"(t));
  return x;
}
__device__ __forceinline__ float xg_sum(float x) { x = swap16_add(x, x); return swap32_add(x, x); }
__device__ __forceinline__ float rdlane(float v, int l) { return __builtin_bit_cast(float, __builtin_amdgcn_readlane(__builtin_bit_cast(int, v), l)); }
__device__ __forceinline__ void wave_sum2(float a, float b, float& sa, float& sb) {
  float v = swap32_add(a, b);
  v += dpp<XOR1>(v); v += dpp<XOR2>(v); v += dpp<XOR7>(v); v += dpp<XOR8>(v);
  v = swap16_add(v, v);
  sa = rdlane(v, 0); sb = rdlane(v, 32);
}
__device__ __forceinline__ float row_sum2(float a, float b) {
  float v = swap32_add(a, b);
  v += dpp<XOR1>(v); v += dpp<XOR2>(v); v += dpp<XOR7>(v); v += dpp<XOR8>(v);
  return v;
}
__device__ __forceinline__ float ln_rden(float var) { return __builtin_amdgcn_rcpf(rbf(__builtin_amdgcn_sqrtf(rbf(rbf(var) + 1e-5f)))); }
__device__ __forceinline__ f32x2 ln_apply2(f32x2 v, float mb, float rden) { return rbf2(rbf2(v - mb) * rden); }
constexpr float GA = -0.4686686930532063f;
constexpr float GK = 4.91222953926424f;
__device__ __forceinline__ f32x2 gelu2v(f32x2 v) {
  const f32x2 z = v * (v * v + GK);
  const f32x2 e1 = f32x2{__builtin_amdgcn_exp2f(z[0]), __builtin_amdgcn_exp2f(z[1])} + 1.0f;
  return v * f32x2{__builtin_amdgcn_rcpf(e1[0]), __builtin_amdgcn_rcpf(e1[1])};
}
__device__ __forceinline__ unsigned umax4(u32x4 v) { return max(max(v[0], v[1]), max(v[2], v[3])); }
__device__ __forceinline__ u64 rt() { return __builtin_amdgcn_s_memrealtime(); }
#define LDS_WAIT() asm volatile("s_waitcnt lgkmcnt(0)" ::: "memory")
#define CFENCE() asm volatile("" ::: "memory")
#define BAR() do { __builtin_amdgcn_sched_barrier(0); LDS_WAIT(); __builtin_amdgcn_s_barrier(); CFENCE(); __builtin_amdgcn_sched_barrier(0); } while (0)
#define LSPIN(cond) do { int sp_ = 0; _Pragma("nounroll") while (cond) { __builtin_amdgcn_s_sleep(1); if (++sp_ > LDS_CAP) break; } __builtin_amdgcn_fence(__ATOMIC_ACQUIRE, "workgroup"); } while (0)

__global__ void __launch_bounds__(512, 2)
k_fused(const u16* __restrict__ x, const u16* __restrict__ K, const u16* __restrict__ V, const u16* __restrict__ wq, const u16* __restrict__ wo,
        const u16* __restrict__ w1, const u16* __restrict__ b1, const u16* __restrict__ w2, const u16* __restrict__ b2, u16* __restrict__ out,
        unsigned char* __restrict__ ws) {
  LAS unsigned char* const L8 = (LAS unsigned char*)0;
  volatile LAS unsigned* CTRL = (volatile LAS unsigned*)(L8 + L_CTRL);
  const int tid = threadIdx.x, lane = tid & 63, wave = __builtin_amdgcn_readfirstlane(tid >> 6), b = blockIdx.x;
  const int g = lane >> 4, c = lane & 15;
  const int kvh = b >> 5, split = b & 31;
  const int bhead = 4 * (b & 7) + (b >> 6), bcg = (b >> 3) & 7;
  const int pj = ((b & 7) << 5) | (b >> 3);
  const __amdgpu_buffer_rsrc_t rs = __builtin_amdgcn_make_buffer_rsrc((void*)ws, 0, W_END, 0x00020000);
  {
    unsigned sq, st, st1, st2, spk, spv, sa, sb, sm0, tile0;
    unsigned vt, va, vc, vv, vk0, vk1, vk2, vk3, vw0, vw1, vw2, vw3, vo0;
#define GL(v, off) "global_load_lds_dwordx4 %[" v "], s[90:91] offset:" off " nt\n\t"
#define PUB(reg, off) "v_mov_b32 %[vt], %[" reg "]\n\tds_write_b32 %[vc], %[vt] offset:" off "\n\t"
    asm volatile(
        "v_readfirstlane_b32 s90, %[tid]\n\t"
        "s_cmp_gt_u32 s90, 63\n\t"
        "s_cbranch_scc1 Lld_end\n\t"
        "s_load_dwordx8 s[64:71], %[ka], 0x0\n\t"
        "s_load_dwordx2 s[72:73], %[ka], 0x20\n\t"
        "s_load_dwordx2 s[74:75], %[ka], 0x50\n\t"
        "s_setprio 3\n\t"
        "v_lshlrev_b32 %[vv], 4, %[tid]\n\t"
        "v_lshrrev_b32 %[vt], 3, %[tid]\n\t"
        "v_and_b32 %[vc], 0x70, %[vv]\n\t"
        "v_lshl_or_b32 %[vw0], %[vt], 15, %[vc]\n\t"
        "v_lshl_or_b32 %[vo0], %[vt], 10, %[vc]\n\t"
        "v_add_u32 %[vw1], 0x1c00, %[vw0]\n\t"
        "v_add_u32 %[vw2], 0x3800, %[vw0]\n\t"
        "v_add_u32 %[vw3], 0x5400, %[vw0]\n\t"
        "s_and_b32 %[sa], %[b], 3\n\t"
        "s_lshl_b32 %[sa], %[sa], 20\n\t"
        "s_lshr_b32 %[sb], %[b], 2\n\t"
        "s_lshl_b32 %[sb], %[sb], 7\n\t"
        "s_add_u32 %[sa], %[sa], %[sb]\n\t"
        "s_mov_b32 m0, %[lx]\n\t"
        "s_mov_b32 %[sb], 4\n\t"
        "s_waitcnt lgkmcnt(0)\n\t"
        "global_load_lds_dwordx4 %[vv], s[64:65]\n\t"
        "s_mov_b32 m0, %[lwo]\n\t"
        "s_add_u32 s90, s70, %[sa]\n\t"
        "s_addc_u32 s91, s71, 0\n\t"
        "Lld_wq:\n\t"
        GL("vw0", "0") GL("vw1", "1024") GL("vw2", "2048") GL("vw3", "3072")
        "s_addk_i32 m0, 0x1000\n\t"
        "s_add_u32 s90, s90, 0x40000\n\t"
        "s_addc_u32 s91, s91, 0\n\t"
        "s_sub_u32 %[sb], %[sb], 1\n\t"
        "s_cmp_lg_u32 %[sb], 0\n\t"
        "s_cbranch_scc1 Lld_wq\n\t"
        "v_and_b32 %[va], 48, %[tid]\n\t"
        "v_lshlrev_b32 %[vt], 1, %[va]\n\t"
        "v_xor_b32 %[vv], %[vv], %[vt]\n\t"
        "v_xor_b32 %[vw0], 0x80, %[vv]\n\t"
        "v_and_b32 %[vt], 15, %[tid]\n\t"
        "v_lshl_or_b32 %[vt], %[vt], 8, %[va]\n\t"
        "v_add_u32 %[vk0], 0xc00, %[vt]\n\t"
        "v_add_u32 %[vk1], 0x840, %[vt]\n\t"
        "v_add_u32 %[vk2], 0x480, %[vt]\n\t"
        "v_add_u32 %[vk3], 0xc0, %[vt]\n\t"
        "s_lshr_b32 %[sa], %[b], 5\n\t"
        "s_lshl_b32 %[sb], %[sa], 24\n\t"
        "s_add_u32 s66, s66, %[sb]\n\t"
        "s_addc_u32 s67, s67, 0\n\t"
        "s_sub_u32 s66, s66, 0xc00\n\t"
        "s_subb_u32 s67, s67, 0\n\t"
        "s_add_u32 s68, s68, %[sb]\n\t"
        "s_addc_u32 s69, s69, 0\n\t"
        "s_and_b32 %[tile0], %[b], 7\n\t"
        "s_lshl_b32 %[tile0], %[tile0], 2\n\t"
        "s_bfe_u32 %[sb], %[b], 0x20003\n\t"
        "s_or_b32 %[tile0], %[tile0], %[sb]\n\t"
        "s_mul_i32 %[tile0], %[tile0], %[tstr]\n\t"
        "s_mov_b32 %[sq], 0\n\t"
        "s_mov_b32 %[spk], 0\n\t"
        "s_mov_b32 %[spv], 0\n\t"
        "s_lshl_b32 %[sa], %[sa], 8\n\t"
        "s_add_u32 %[sa], %[sa], %[wpool]\n\t"
        "s_add_u32 s74, s74, %[sa]\n\t"
        "s_addc_u32 s75, s75, 0\n\t"
        "s_and_b32 %[sa], %[b], 7\n\t"
        "s_lshl_b32 %[sa], %[sa], 2\n\t"
        "s_lshr_b32 %[sb], %[b], 6\n\t"
        "s_add_u32 %[sa], %[sa], %[sb]\n\t"
        "s_lshl_b32 %[sa], %[sa], 17\n\t"
        "s_and_b32 %[sb], %[b], 0x38\n\t"
        "s_lshl_b32 %[sb], %[sb], 4\n\t"
        "s_add_u32 %[sa], %[sa], %[sb]\n\t"
        "s_add_u32 s72, s72, %[sa]\n\t"
        "s_addc_u32 s73, s73, 0\n\t"
        "v_mov_b32 %[vc], %[ctrl]\n\t"
        "v_lshl_add_u32 %[vt], %[tid], 2, %[vc]\n\t"
        "v_mov_b32 %[va], 0\n\t"
        "ds_write_b32 %[vt], %[va]\n\t"
        "v_bfrev_b32 %[va], -2\n\t"
        "ds_write_b32 %[vc], %[va] offset:8\n\t"
        "Lld_loop:\n\t"
        "s_cmp_lt_u32 %[sq], %[nstat]-2\n\t"
        "s_cbranch_scc1 Lld_noclaim\n\t"
        "s_add_u32 %[sa], %[sq], 2\n\t"
        "s_lshl_b64 exec, 1, %[sa]\n\t"
        "v_mov_b32 %[vt], 0\n\t"
        "v_mov_b32 %[va], 1\n\t"
        "global_atomic_add %[va], %[vt], %[va], s[74:75] sc0\n\t"
        "s_mov_b64 exec, -1\n\t"
        "Lld_noclaim:\n\t"
        "s_add_u32 %[st], %[tile0], %[sq]\n\t"
        "s_cmp_lt_u32 %[sq], %[nstat]\n\t"
        "s_cbranch_scc1 Lld_havetile\n\t"
        "v_readlane_b32 %[sa], %[va], %[sq]\n\t"
        "s_nop 0\n\t"
        "s_cmp_ge_u32 %[sa], %[npool]\n\t"
        "s_cbranch_scc1 Lld_tail\n\t"
        "s_cmp_ge_u32 %[sq], %[nstat]+%[npool]\n\t"
        "s_cbranch_scc1 Lld_tail\n\t"
        "s_and_b32 %[st], %[sa], 31\n\t"
        "s_lshl_b32 %[st], %[st], 6\n\t"
        "s_lshr_b32 %[sa], %[sa], 5\n\t"
        "s_add_u32 %[st], %[st], %[sa]\n\t"
        "s_add_u32 %[st], %[st], %[nstat]\n\t"
        "Lld_havetile:\n\t"
        "s_cmp_lt_u32 %[sq], 8\n\t"
        "s_cbranch_scc1 Lld_free\n\t"
        "s_and_b32 %[sa], %[sq], 7\n\t"
        "s_lshl_b32 %[sa], %[sa], 2\n\t"
        "s_mov_b32 %[sb], %[cap]\n\t"
        "Lld_spin:\n\t"
        "v_add_u32 %[vt], %[sa], %[vc]\n\t"
        "ds_read_b32 %[vt], %[vt] offset:64\n\t"
        "s_waitcnt lgkmcnt(0)\n\t"
        "v_readfirstlane_b32 %[sm0], %[vt]\n\t"
        "s_nop 0\n\t"
        "s_add_u32 %[sm0], %[sm0], 7\n\t"
        "s_cmp_ge_u32 %[sm0], %[sq]\n\t"
        "s_cbranch_scc1 Lld_free\n\t"
        "s_waitcnt vmcnt(0)\n\t"
        "s_mov_b32 %[spk], %[sq]\n\t"
        "s_sub_u32 %[spv], %[sq], 2\n\t"
        PUB("spk", "0") PUB("spv", "4")
        "s_sleep 1\n\t"
        "s_sub_u32 %[sb], %[sb], 1\n\t"
        "s_cmp_lg_u32 %[sb], 0\n\t"
        "s_cbranch_scc1 Lld_spin\n\t"
        "Lld_free:\n\t"
        "s_lshl_b32 %[sa], %[st], 13\n\t"
        "s_add_u32 s90, s66, %[sa]\n\t"
        "s_addc_u32 s91, s67, 0\n\t"
        "s_and_b32 %[sa], %[sq], 7\n\t"
        "s_lshl_b32 m0, %[sa], 14\n\t"
        "s_nop 0\n\t"
        GL("vk0", "0") GL("vk1", "1024") GL("vk2", "2048") GL("vk3", "3072")
        "s_addk_i32 m0, 0x1000\n\t"
        "s_add_u32 s90, s90, 0x1000\n\t"
        "s_addc_u32 s91, s91, 0\n\t"
        GL("vk0", "0") GL("vk1", "1024") GL("vk2", "2048") GL("vk3", "3072")
        "s_cmp_lt_u32 %[sq], 2\n\t"
        "s_cbranch_scc1 Lld_next\n\t"
        "s_waitcnt vmcnt(32)\n\t"
        "s_cmp_lg_u32 %[sq], 2\n\t"
        "s_cbranch_scc1 Lld_nobar\n\t"
        "s_waitcnt vmcnt(24)\n\t"
        "s_waitcnt lgkmcnt(0)\n\t"
        "s_barrier\n\t"
        "Lld_nobar:\n\t"
        "s_cmp_lt_u32 %[sq], 4\n\t"
        "s_cbranch_scc1 Lld_nopk\n\t"
        "s_sub_i32 %[sa], %[sq], 1\n\t"
        "s_max_i32 %[spk], %[spk], %[sa]\n\t"
        PUB("spk", "0")
        "Lld_nopk:\n\t"
        "s_lshl_b32 %[sa], %[st2], 13\n\t"
        "s_add_u32 s90, s68, %[sa]\n\t"
        "s_addc_u32 s91, s69, 0\n\t"
        "s_sub_u32 %[sa], %[sq], 2\n\t"
        "s_and_b32 %[sa], %[sa], 7\n\t"
        "s_lshl_b32 %[sa], %[sa], 14\n\t"
        "s_add_u32 m0, %[sa], 0x2000\n\t"
        "s_nop 0\n\t"
        GL("vv", "0") GL("vw0", "1024") GL("vv", "2048") GL("vw0", "3072")
        "s_addk_i32 m0, 0x1000\n\t"
        "s_add_u32 s90, s90, 0x1000\n\t"
        "s_addc_u32 s91, s91, 0\n\t"
        GL("vv", "0") GL("vw0", "1024") GL("vv", "2048") GL("vw0", "3072")
        "s_cmp_eq_u32 %[sq], 2\n\t"
        "s_cbranch_scc1 Lld_next\n\t"
        "s_waitcnt vmcnt(32)\n\t"
        "s_sub_i32 %[sa], %[sq], 3\n\t"
        "s_max_i32 %[spv], %[spv], %[sa]\n\t"
        PUB("spv", "4")
        "Lld_next:\n\t"
        "s_mov_b32 %[st2], %[st1]\n\t"
        "s_mov_b32 %[st1], %[st]\n\t"
        "s_add_u32 %[sq], %[sq], 1\n\t"
        "s_branch Lld_loop\n\t"
        "Lld_tail:\n\t"
        PUB("sq", "8")
        "s_mov_b32 %[sb], 2\n\t"
        "Lld_tail2:\n\t"
        "s_lshl_b32 %[sa], %[st2], 13\n\t"
        "s_add_u32 s90, s68, %[sa]\n\t"
        "s_addc_u32 s91, s69, 0\n\t"
        "s_sub_u32 %[sa], %[sq], %[sb]\n\t"
        "s_and_b32 %[sa], %[sa], 7\n\t"
        "s_lshl_b32 %[sa], %[sa], 14\n\t"
        "s_add_u32 m0, %[sa], 0x2000\n\t"
        "s_nop 0\n\t"
        GL("vv", "0") GL("vw0", "1024") GL("vv", "2048") GL("vw0", "3072")
        "s_addk_i32 m0, 0x1000\n\t"
        "s_add_u32 s90, s90, 0x1000\n\t"
        "s_addc_u32 s91, s91, 0\n\t"
        GL("vv", "0") GL("vw0", "1024") GL("vv", "2048") GL("vw0", "3072")
        "s_mov_b32 %[st2], %[st1]\n\t"
        "s_sub_u32 %[sb], %[sb], 1\n\t"
        "s_cmp_lg_u32 %[sb], 0\n\t"
        "s_cbranch_scc1 Lld_tail2\n\t"
        "s_waitcnt vmcnt(40)\n\t"
        "s_sub_i32 %[sa], %[sq], 1\n\t"
        PUB("sa", "0")
        "s_waitcnt vmcnt(32)\n\t"
        "s_sub_i32 %[sa], %[sq], 3\n\t"
        "s_max_i32 %[spv], %[spv], %[sa]\n\t"
        PUB("spv", "4")
        "v_add_u32 %[vw1], 0x1c00, %[vo0]\n\t"
        "v_add_u32 %[vw2], 0x3800, %[vo0]\n\t"
        "v_add_u32 %[vw3], 0x5400, %[vo0]\n\t"
        "s_mov_b32 s90, s72\n\t"
        "s_mov_b32 s91, s73\n\t"
        "s_mov_b32 m0, %[lwo]\n\t"
        "s_waitcnt vmcnt(24)\n\t"
        PUB("sq", "0")
#define WO4 GL("vo0", "0") GL("vw1", "1024") GL("vw2", "2048") GL("vw3", "3072") "s_addk_i32 m0, 0x1000\n\ts_add_u32 s90, s90, 0x8000\n\ts_addc_u32 s91, s91, 0\n\t"
        "s_sub_u32 %[spv], %[sq], 2\n\t"
        "s_waitcnt vmcnt(16)\n\t"
        PUB("spv", "4")
        "s_add_u32 %[spv], %[spv], 1\n\t"
        "s_waitcnt vmcnt(8)\n\t"
        PUB("spv", "4")
        "s_waitcnt vmcnt(0)\n\t"
        PUB("sq", "4")
        WO4 WO4 WO4 WO4
        "s_waitcnt lgkmcnt(0)\n\t"
        "s_barrier\n\t"
        "s_waitcnt vmcnt(0)\n\t"
        PUB("sq", "28")
        "s_waitcnt lgkmcnt(0)\n\t"
        "s_endpgm\n\t"
        "Lld_end:"
        : [sq] "=&s"(sq), [st] "=&s"(st), [st1] "=&s"(st1), [st2] "=&s"(st2), [spk] "=&s"(spk), [spv] "=&s"(spv), [sa] "=&s"(sa), [sb] "=&s"(sb), [sm0] "=&s"(sm0),
          [tile0] "=&s"(tile0), [vt] "=&v"(vt), [va] "=&v"(va), [vc] "=&v"(vc), [vv] "=&v"(vv), [vk0] "=&v"(vk0), [vk1] "=&v"(vk1), [vk2] "=&v"(vk2), [vk3] "=&v"(vk3),
          [vw0] "=&v"(vw0), [vw1] "=&v"(vw1), [vw2] "=&v"(vw2), [vw3] "=&v"(vw3), [vo0] "=&v"(vo0)
        : [tid] "v"(tid), [b] "s"(b), [ka] "s"(__builtin_amdgcn_kernarg_segment_ptr()), [ctrl] "i"(L_CTRL), [nstat] "i"(NSTAT), [npool] "i"(NPOOL), [cap] "i"(LDS_CAP),
          [lx] "i"(L_MRG), [lwo] "i"(L_WO), [wpool] "i"(W_POOL), [tstr] "i"(NT)
        : "memory", "scc", "s64", "s65", "s66", "s67", "s68", "s69", "s70", "s71", "s72", "s73", "s74", "s75", "s90", "s91");
  }
  if (wave <= 4) {
    BAR();
    const u32x4 xv = *(const LAS u32x4*)(L8 + L_MRG + lane * 16);
    const u32x2 xo = *(const LAS u32x2*)(L8 + L_MRG + ((split & 3) * 128 + 32 * (wave - 1) + 4 * (lane >> 3)) * 2);
    u32x4 wqv[4];
#pragma unroll
    for (int i = 0; i < 4; ++i) wqv[i] = *(const LAS u32x4*)(L8 + L_WO + (wave - 1) * 4096 + i * 1024 + lane * 16);
    constexpr unsigned ONES = 0x3f803f80u;
    float S, Q;
    wave_sum2(dot2(xv[0], ONES, dot2(xv[1], ONES, 0.f)) + dot2(xv[2], ONES, dot2(xv[3], ONES, 0.f)),
              dot2(xv[0], xv[0], dot2(xv[1], xv[1], 0.f)) + dot2(xv[2], xv[2], dot2(xv[3], xv[3], 0.f)), S, Q);
    const float md = S * (1.0f / D);
    const float mb = rbf(md), rden = ln_rden(fmaxf(fmaf(-md, md, Q * (1.0f / D)), 0.f));
    const f32x2 xa = ln_apply2(unpk(xo[0]), mb, rden), xb = ln_apply2(unpk(xo[1]), mb, rden);
    f32x2 a2[4];
#pragma unroll
    for (int j = 0; j < 4; ++j) a2[j] = (unpk(wqv[0][j]) * xa[0] + unpk(wqv[1][j]) * xa[1]) + (unpk(wqv[2][j]) * xb[0] + unpk(wqv[3][j]) * xb[1]);
    float a[8];
#pragma unroll
    for (int j = 0; j < 8; ++j) a[j] = a2[j >> 1][j & 1];
#pragma unroll
    for (int j = 0; j < 4; ++j) a[j] = swap32_add(a[j], a[j + 4]);
#pragma unroll
    for (int j = 0; j < 2; ++j) a[j] = swap16_add(a[j], a[j + 2]);
    { const float keep = (lane & 8) ? a[1] : a[0], send = (lane & 8) ? a[0] : a[1]; a[0] = keep + dpp<XOR8>(send); }
    ((LAS float*)(L8 + L_QP))[(wave - 1) * 64 + 8 * (lane & 7) + (lane >> 3)] = a[0];
    LDS_WAIT();
    if (lane == 0) __hip_atomic_fetch_add((LAS unsigned*)(L8 + L_CTRL) + C_QCNT, 1u, __ATOMIC_RELAXED, __HIP_MEMORY_SCOPE_WORKGROUP);
  } else if (wave == HELPER) {
    const u64 epoch = __hip_atomic_load((GAS u64*)(ws + W_EPOCH), __ATOMIC_RELAXED, __HIP_MEMORY_SCOPE_AGENT);
    const u32x4 sv = {SENT, SENT, SENT, SENT};
    if (lane < 40) {
#pragma unroll
      for (int h = 0; h < 4; ++h) __builtin_amdgcn_raw_buffer_store_b128(sv, rs, W_PA + (((4 * kvh + h) * 5 + (lane >> 3)) * NSPLIT + split) * 128 + (lane & 7) * 16, 0, AUX_SC1);
    }
    if (lane < 24) __builtin_amdgcn_raw_buffer_store_b128(sv, rs, lane < 16 ? W_Y + (bhead * D + bcg * 64) * 4 + lane * 16 : W_Y16 + (bhead * D + bcg * 64) * 2 + (lane - 16) * 16, 0, AUX_SC1);
    if (split == 0 && lane == 0) __builtin_amdgcn_raw_buffer_store_b32(0u, rs, W_POOL + kvh * 256, 0, AUX_SC1);
    BAR();
    asm volatile("s_waitcnt vmcnt(0)" ::: "memory");
    const u64 tag = (epoch + 1) << 24;
    const unsigned tlo = (unsigned)tag, thi = (unsigned)(tag >> 32);
    LSPIN(CTRL[C_QCNT] < 4u);
    {
      const LAS float* qp = (const LAS float*)(L8 + L_QP) + lane;
      const unsigned qs = __float_as_uint((qp[0] + qp[64]) + (qp[128] + qp[192]));
      __hip_atomic_store((GAS u64*)(ws + W_QG) + (kvh * 4 + (split & 3)) * 512 + (split >> 2) * 64 + lane, tag | (u64)((qs + 0x80u) >> 8), __ATOMIC_RELAXED, __HIP_MEMORY_SCOPE_AGENT);
    }
    if (lane == 0) __hip_atomic_store((GAS u64*)(ws + W_FLAG) + b, epoch + 1, __ATOMIC_RELAXED, __HIP_MEMORY_SCOPE_AGENT);
    { const u64 tp = rt(); _Pragma("nounroll") while (rt() - tp < QD) __builtin_amdgcn_s_sleep(2); }
    {
      u32x4 gq[16];
      _Pragma("nounroll") for (int sp = 0; sp < SPIN_CAP; ++sp) { CFENCE();
#pragma unroll
        for (int k = 0; k < 16; ++k) gq[k] = __builtin_amdgcn_raw_buffer_load_b128(rs, W_QG + kvh * 16384 + k * 1024 + lane * 16, 0, AUX_SC1);
        unsigned bad = 0;
#pragma unroll
        for (int k = 0; k < 16; ++k) bad |= (((gq[k][0] ^ tlo) | (gq[k][2] ^ tlo)) & 0xff000000u) | (gq[k][1] ^ thi) | (gq[k][3] ^ thi);
        if (__all(bad == 0u)) break;
        __builtin_amdgcn_s_sleep(4);
      }
#pragma unroll
      for (int k = 0; k < 4; ++k) {
        const float lo = (__uint_as_float(gq[k][0] << 8) + __uint_as_float(gq[4 + k][0] << 8)) + (__uint_as_float(gq[8 + k][0] << 8) + __uint_as_float(gq[12 + k][0] << 8));
        const float hi = (__uint_as_float(gq[k][2] << 8) + __uint_as_float(gq[4 + k][2] << 8)) + (__uint_as_float(gq[8 + k][2] << 8) + __uint_as_float(gq[12 + k][2] << 8));
        *(LAS unsigned*)(L8 + L_QS + k * 256 + lane * 4) = pk_bf16(lo, hi);
      }
      LDS_WAIT();
      CTRL[C_QREADY] = 1u;
    }
    LSPIN(CTRL[C_KL] < (unsigned)(NT / 2));
    _Pragma("nounroll") for (int sp = 0; sp < SPIN_CAP; ++sp) { CFENCE();
      const u32x4 t0 = __builtin_amdgcn_raw_buffer_load_b128(rs, W_FLAG + lane * 16, 0, AUX_SC1), t1 = __builtin_amdgcn_raw_buffer_load_b128(rs, W_FLAG + 1024 + lane * 16, 0, AUX_SC1);
      const unsigned el = (unsigned)(epoch + 1), eh = (unsigned)((epoch + 1) >> 32);
      const unsigned bad = (t0[0] ^ el) | (t0[2] ^ el) | (t1[0] ^ el) | (t1[2] ^ el) | (t0[1] ^ eh) | (t0[3] ^ eh) | (t1[1] ^ eh) | (t1[3] ^ eh);
      if (__all(bad == 0u)) break;
      __builtin_amdgcn_s_sleep(8);
    }
    if (b == 0 && lane == 0) __hip_atomic_store((GAS u64*)(ws + W_EPOCH), epoch + 1, __ATOMIC_RELAXED, __HIP_MEMORY_SCOPE_AGENT);
    asm volatile("s_waitcnt vmcnt(0)" ::: "memory");
  } else {
    BAR();
  }

  const int tw = wave & 3;
  unsigned pr[12], xr = 0, pbb = 0;
  if (wave >= 4) {
    const int t = tid - 256;
#pragma unroll
    for (int i = 0; i < 3; ++i) {
      const u16* pp = i == 0 ? w1 : i == 1 ? b1 : w2;
      const u32x2 f4 = *(const GAS u32x2*)(pp + 4 * t);
      pr[4 * i] = f4[0]; pr[4 * i + 1] = f4[1]; pr[4 * i + 2] = *(const GAS unsigned*)(pp + 1024 + 2 * t); pr[4 * i + 3] = pp[1536 + t];
    }
    xr = *(const GAS unsigned*)(x + 128 * tw + 8 * (lane & 15) + 2 * (lane >> 4));
    pbb = *(const GAS u16*)b2;
  } else {
#pragma unroll
    for (int i = 0; i < 12; ++i) pr[i] = 0;
    xr = *(const GAS unsigned*)(x + 2 * pj);
  }
  if (wave != HELPER) {
    f32x4 o[8];
#pragma unroll
    for (int d = 0; d < 8; ++d) o[d] = f32x4{0.f, 0.f, 0.f, 0.f};
    float m = -INFINITY, l = 0.f;
    u32x4 qf[4];
#pragma unroll
    for (int s = 0; s < 4; ++s) qf[s] = u32x4{0u, 0u, 0u, 0u};
    constexpr float SCALE = 0.08838834764831845f;
#define TICKET() __builtin_amdgcn_readfirstlane(lane == 0 ? __hip_atomic_fetch_add((LAS unsigned*)(L8 + L_CTRL) + C_TICKET, 1u, __ATOMIC_RELAXED, __HIP_MEMORY_SCOPE_WORKGROUP) : 0u)
    const LAS unsigned char* ring = L8 + L_RING + lane * 16;
    const int vbase = L_RING + 8192 + (4 * g + (c >> 2)) * 256 + ((((c & 3) >> 1) ^ (2 * (4 * (g & 1) + (c >> 2)))) << 4) + (c & 1) * 8;
    const int ci = wave > HELPER ? wave - 2 : wave - 1;
    int a_mrec = L_MRG + (ci * 4 + c) * 528 + g * 16, a_mrml = L_MML + c * 64 + ci * 8;
    int a_ctrl = L_CTRL;
    asm volatile("" : "+v"(a_mrec), "+v"(a_mrml), "+v"(a_ctrl));
#define V_FRAGS() _Pragma("unroll") for (int j = 0; j < 8; ++j) { \
        const u32x2 lo_ = __builtin_bit_cast(u32x2, __builtin_amdgcn_ds_read_tr16_b64_v4i16((LAS s16x4*)(L8 + va[j]))); \
        const u32x2 hi_ = __builtin_bit_cast(u32x2, __builtin_amdgcn_ds_read_tr16_b64_v4i16((LAS s16x4*)(L8 + va[j] + 4096))); \
        tv[j] = u32x4{lo_[0], lo_[1], hi_[0], hi_[1]}; }
    unsigned q = TICKET();
    for (int it = 0;; ++it) {
      u32x4 tk[8], tv[8];
      const int s = q & (NSLOT - 1);
      const LAS unsigned char* sp = ring + s * SLOT;
      int va[8];
#pragma unroll
      for (int j = 0; j < 8; ++j) { va[j] = (vbase + s * SLOT) ^ (j << 5); asm volatile("" : "+v"(va[j])); }
      unsigned ntot;
#define WAIT_LANDED(idx) do { int sp_ = 0; _Pragma("nounroll") for (;;) { const u32x4 cw_ = *(const volatile LAS u32x4*)(L8 + L_CTRL); \
        ntot = __builtin_amdgcn_readfirstlane(cw_[C_NTOT]); \
        if (__builtin_amdgcn_readfirstlane(cw_[idx]) > q || ntot <= q || ++sp_ > LDS_CAP) break; __builtin_amdgcn_s_sleep(1); } \
        __builtin_amdgcn_fence(__ATOMIC_ACQUIRE, "workgroup"); } while (0)
#define WAIT_V() do { u32x2 cw_; unsigned vl_, cnt_; \
        asm volatile("s_mov_b32 %[cnt], %[cap]\n\t1:\n\tds_read2_b32 %[cw], %[ad] offset0:1 offset1:2\n\ts_waitcnt lgkmcnt(0)\n\t" \
                     "v_readfirstlane_b32 %[vl], v150\n\tv_readfirstlane_b32 %[nt], v151\n\ts_sub_u32 %[cnt], %[cnt], 1\n\ts_cbranch_scc1 2f\n\t" \
                     "s_cmp_le_u32 %[vl], %[q]\n\ts_cbranch_scc1 1b\n\t2:" \
                     : [cw] "={v[150:151]}"(cw_), [vl] "=&s"(vl_), [nt] "=&s"(ntot), [cnt] "=&s"(cnt_) : [ad] "v"(a_ctrl), [q] "s"(q), [cap] "i"(LDS_CAP) : "memory", "scc"); } while (0)
#define READ_V() do { WAIT_V(); \
      V_FRAGS() \
      LDS_WAIT(); CTRL[C_FREE + s] = q + 1; \
      asm volatile("" : "+v"(tv[0]), "+v"(tv[1]), "+v"(tv[2]), "+v"(tv[3]), "+v"(tv[4]), "+v"(tv[5]), "+v"(tv[6]), "+v"(tv[7]) :: "memory"); } while (0)
      WAIT_LANDED(C_KL);
      if (ntot <= q || q >= (unsigned)(NT + NPOOL)) break;
#pragma unroll
      for (int j = 0; j < 8; ++j) tk[j] = *(const LAS u32x4*)(sp + j * 1024);
      if (it == 0) {
        READ_V();
        LSPIN(CTRL[C_QREADY] == 0u);
        if (c < 4) {
#pragma unroll
          for (int s2 = 0; s2 < 4; ++s2) qf[s2] = *(const LAS u32x4*)(L8 + L_QS + c * 256 + s2 * 64 + g * 16);
        }
      }
      f32x4 s0 = {0.f, 0.f, 0.f, 0.f}, s1 = {0.f, 0.f, 0.f, 0.f};
#pragma unroll
      for (int k = 0; k < 4; ++k) { s0 = mfma16(tk[k], qf[k], s0); s1 = mfma16(tk[4 + k], qf[k], s1); }
      unsigned qn = 0x7fffffffu;
      if (wave < 4 || q < (unsigned)(NSTAT - RETIRE)) qn = TICKET();
      f32x2 sc[4];
      sc[0] = rbf2(rbf2(f32x2{s0[0], s0[1]}) * SCALE); sc[1] = rbf2(rbf2(f32x2{s0[2], s0[3]}) * SCALE);
      sc[2] = rbf2(rbf2(f32x2{s1[0], s1[1]}) * SCALE); sc[3] = rbf2(rbf2(f32x2{s1[2], s1[3]}) * SCALE);
#pragma unroll
      for (int r = 0; r < 4; ++r) sc[r] *= LOG2E;
      const float mx = xg_max2(vmax3(vmax3(sc[0][0], sc[0][1], sc[1][0]), vmax3(sc[1][1], sc[2][0], sc[2][1]), vmax(sc[3][0], sc[3][1])));
      if (__any(mx > m)) {
        const float mn = vmax(m, __builtin_ceilf(mx));
        const float alpha = __builtin_amdgcn_exp2f(m - mn);
        m = mn; l *= alpha;
#pragma unroll
        for (int d = 0; d < 8; ++d) o[d] *= alpha;
      }
      float p[8];
#pragma unroll
      for (int r = 0; r < 8; ++r) p[r] = __builtin_amdgcn_exp2f(sc[r >> 1][r & 1] - m);
      l += xg_sum(((p[0] + p[1]) + (p[2] + p[3])) + ((p[4] + p[5]) + (p[6] + p[7])));
      u32x4 pf = {pk_bf16(p[0], p[1]), pk_bf16(p[2], p[3]), pk_bf16(p[4], p[5]), pk_bf16(p[6], p[7])};
      asm volatile("" : "+v"(pf), "+v"(l));
      if (it != 0) {
        WAIT_V();
        V_FRAGS()
      }
#pragma unroll
      for (int d = 0; d < 8; ++d) o[d] = mfma16(tv[d], pf, o[d]);
      if (qn >= ntot && wave < 4) break;
      if (it != 0) { LDS_WAIT(); CTRL[C_FREE + s] = q + 1; }
      if (qn >= ntot) break;
      q = qn;
    }
    if (c < 4) {
      *(LAS f32x2*)(L8 + a_mrml) = f32x2{m, l};
#pragma unroll
      for (int d = 0; d < 8; ++d) *(LAS f32x4*)(L8 + a_mrec + d * 64) = o[d];
    }
  }
  if (wave < 4) {
    BAR();
    if (wave != 1) return;
    const int o_own = W_Y + ((lane & 31) * D + 2 * pj + (lane >> 5)) * 4;
    BAR();
    { const u64 tp = rt(); _Pragma("nounroll") while (rt() - tp < CD + 5) __builtin_amdgcn_s_sleep(1); }
    unsigned yo = 0;
    _Pragma("nounroll") for (int sp = 0; sp < SPIN_CAP; ++sp) { CFENCE();
      yo = __builtin_amdgcn_raw_buffer_load_b32(rs, o_own, 0, AUX_SC1);
      if (!__any(yo == SENT)) break;
      __builtin_amdgcn_s_sleep(1);
    }
    float v = __uint_as_float(yo);
    v += dpp<XOR1>(v); v += dpp<XOR2>(v); v += dpp<XOR7>(v); v += dpp<XOR8>(v);
    v = swap16_add(v, v);
    *(LAS float*)(L8 + L_STAT + 64 + (lane >> 5) * 4) = rbf(((lane >> 5) ? bfhi(xr) : bf2f(xr)) + rbf(v));
    BAR();
    return;
  }

  __builtin_amdgcn_s_setprio(2);
  float w1f[7], b1f[7], w2f[7];
#pragma unroll
  for (int i = 0; i < 7; ++i) { w1f[i] = unpk(pr[i >> 1])[i & 1] * GA; b1f[i] = unpk(pr[4 + (i >> 1)])[i & 1] * GA; w2f[i] = unpk(pr[8 + (i >> 1)])[i & 1] * (1.0f / GA); asm volatile("" : "+v"(w1f[i]), "+v"(b1f[i]), "+v"(w2f[i])); }
  const int hb = lane >> 5;
  int a_rec = L_MRG + (hb * 12 + tw) * 528 + (lane & 31) * 16;
  int a_mml = L_MML + tw * 64 + hb * 24;
  int o_pa = W_PA + (((4 * kvh + tw) * 5 + (lane >> 3)) * NSPLIT + split) * 128 + (lane & 7) * 16;
  int o_bm = W_PA + (bhead * 5 + 4) * 4096 + (c >> 1) * 128 + (2 * g + (c & 1)) * 16, o_bd = o_bm - (4 - tw) * 4096;
  int o_y = W_Y + (bhead * D + bcg * 64 + 4 * c) * 4;
  int o_c = W_Y16 + (g * D + 128 * tw + 8 * c) * 2;
  int o_y16 = W_Y16 + (bhead * D + bcg * 64 + 4 * c) * 2;
  int a_st = L_STAT + tw * 16 + g * 4, a_fin = L_STAT;
  int a_ob = L_YP + tw * 256 + g * 16, a_yr = L_YP + c * 16;
  f32x2 xf = unpk(xr); float bbf = bf2f(pbb);
  unsigned sel_lo = 0x00003f80u, sel_hi = 0x3f800000u;
  asm volatile("" : "+v"(sel_lo), "+v"(sel_hi));
  asm volatile("" : "+v"(a_rec), "+v"(a_mml), "+v"(o_pa), "+v"(o_bd), "+v"(o_bm), "+v"(o_y), "+v"(o_c), "+v"(o_y16), "+v"(a_st), "+v"(a_fin), "+v"(a_ob), "+v"(a_yr), "+v"(xf), "+v"(bbf));
  BAR();
  {
    const f32x2 m0 = *(const LAS f32x2*)(L8 + a_mml), m1 = *(const LAS f32x2*)(L8 + a_mml + 8), m2 = *(const LAS f32x2*)(L8 + a_mml + 16);
    const f32x4 r0 = *(const LAS f32x4*)(L8 + a_rec), r1 = *(const LAS f32x4*)(L8 + a_rec + 2112), r2 = *(const LAS f32x4*)(L8 + a_rec + 4224);
    float M = vmax3(m0[0], m1[0], m2[0]), t;
    asm("v_mov_b32 %1, %0\n\ts_nop 1\n\tv_permlane32_swap_b32 %0, %1\n\tv_max_f32 %0, %0, %1" : "+v"(M), "=&v"(t));
    const float e0 = __builtin_amdgcn_exp2f(m0[0] - M), e1 = __builtin_amdgcn_exp2f(m1[0] - M), e2 = __builtin_amdgcn_exp2f(m2[0] - M);
    const float Lp = e0 * m0[1] + e1 * m1[1] + e2 * m2[1];
    const f32x4 acc = r0 * e0 + r1 * e1 + r2 * e2;
    u32x4 ub;
    ub[0] = __float_as_uint(swap32_add(acc[0], 0.5f * M));
    ub[1] = __float_as_uint(swap32_add(acc[1], Lp));
    ub[2] = __float_as_uint(swap32_add(acc[2], Lp));
    ub[3] = __float_as_uint(swap32_add(acc[3], Lp));
#pragma unroll
    for (int k = 0; k < 4; ++k) ub[k] = min(ub[k], SENT - 1u);
    if (lane < 40) __builtin_amdgcn_raw_buffer_store_b128(ub, rs, o_pa, 0, AUX_SC1);
  }
  LSPIN(CTRL[C_WO] == 0u);
  u32x4 wof[4];
#pragma unroll
  for (int s = 0; s < 4; ++s) {
    const int a = L_WO + (32 * tw + 8 * g + (c >> 2)) * 128 + s * 32 + (c & 3) * 8;
    const u32x2 lo = __builtin_bit_cast(u32x2, __builtin_amdgcn_ds_read_tr16_b64_v4i16((LAS s16x4*)(L8 + a)));
    const u32x2 hi = __builtin_bit_cast(u32x2, __builtin_amdgcn_ds_read_tr16_b64_v4i16((LAS s16x4*)(L8 + a + 512)));
    wof[s] = u32x4{lo[0], lo[1], hi[0], hi[1]};
  }
  {
    u32x4 a[4]; u32x2 mlv[4];
    {
      u64 k0, k1, k2, k3; unsigned cnt, t0, t1;
#define B_LD(k, off) "s_mov_b64 exec, %[k" k "]\n\tbuffer_load_dwordx2 %[m" k "], %[om], %[rs], 0 offen offset:" off " sc1\n\tbuffer_load_dwordx4 %[a" k "], %[od], %[rs], 0 offen offset:" off " sc1\n\t"
      asm volatile(
          "s_mov_b64 %[k0], -1\n\ts_mov_b64 %[k1], -1\n\ts_mov_b64 %[k2], -1\n\ts_mov_b64 %[k3], -1\n\ts_mov_b32 %[cnt], %[cap]\n\t"
          "Lb_poll:\n\t"
          B_LD("0", "0") B_LD("1", "1024") B_LD("2", "2048") B_LD("3", "3072")
          "s_mov_b64 exec, -1\n\t"
          "s_waitcnt vmcnt(6)\n\t"
          "v_max3_u32 %[t0], v100, v101, v102\n\tv_max3_u32 %[t0], %[t0], v103, v116\n\tv_max_u32 %[t0], %[t0], v117\n\t"
          "s_waitcnt vmcnt(4)\n\t"
          "v_max3_u32 %[t1], v104, v105, v106\n\tv_cmp_eq_u32 %[k0], -1, %[t0]\n\tv_max3_u32 %[t1], %[t1], v107, v118\n\tv_max_u32 %[t1], %[t1], v119\n\t"
          "s_waitcnt vmcnt(2)\n\t"
          "v_max3_u32 %[t0], v108, v109, v110\n\tv_cmp_eq_u32 %[k1], -1, %[t1]\n\tv_max3_u32 %[t0], %[t0], v111, v120\n\tv_max_u32 %[t0], %[t0], v121\n\t"
          "s_waitcnt vmcnt(0)\n\t"
          "v_max3_u32 %[t1], v112, v113, v114\n\tv_cmp_eq_u32 %[k2], -1, %[t0]\n\tv_max3_u32 %[t1], %[t1], v115, v122\n\tv_max_u32 %[t1], %[t1], v123\n\t"
          "s_or_b64 vcc, %[k0], %[k1]\n\t"
          "v_cmp_eq_u32 %[k3], -1, %[t1]\n\t"
          "s_or_b64 vcc, vcc, %[k2]\n\t"
          "s_sub_u32 %[cnt], %[cnt], 1\n\t"
          "s_cbranch_scc1 Lb_done\n\t"
          "s_or_b64 vcc, vcc, %[k3]\n\t"
          "s_cbranch_scc1 Lb_poll\n\t"
          "Lb_done:"
          : [a0] "={v[100:103]}"(a[0]), [a1] "={v[104:107]}"(a[1]), [a2] "={v[108:111]}"(a[2]), [a3] "={v[112:115]}"(a[3]),
            [m0] "={v[116:117]}"(mlv[0]), [m1] "={v[118:119]}"(mlv[1]), [m2] "={v[120:121]}"(mlv[2]), [m3] "={v[122:123]}"(mlv[3]),
            [k0] "=&s"(k0), [k1] "=&s"(k1), [k2] "=&s"(k2), [k3] "=&s"(k3), [cnt] "=&s"(cnt), [t0] "=&v"(t0), [t1] "=&v"(t1)
          : [om] "{v124}"(o_bm), [od] "{v125}"(o_bd), [rs] "s"(rs), [cap] "i"(SPIN_CAP)
          : "memory", "vcc", "scc");
    }
#define MF(k, i) __uint_as_float(mlv[k][i])
    float M = vmax(vmax3(MF(0, 0), MF(1, 0), MF(2, 0)), MF(3, 0));
    asm("s_nop 1\n\tv_max_f32_dpp %0, %0, %0 row_ror:8 row_mask:0xf bank_mask:0xf\n\ts_nop 1\n\tv_max_f32_dpp %0, %0, %0 row_ror:4 row_mask:0xf bank_mask:0xf\n\t"
        "s_nop 1\n\tv_max_f32_dpp %0, %0, %0 row_ror:2 row_mask:0xf bank_mask:0xf" : "+v"(M));
    const float w0 = __builtin_amdgcn_exp2f(MF(0, 0) - M), w1_ = __builtin_amdgcn_exp2f(MF(1, 0) - M), w2_ = __builtin_amdgcn_exp2f(MF(2, 0) - M), w3 = __builtin_amdgcn_exp2f(MF(3, 0) - M);
    float L = (w0 * MF(0, 1) + w1_ * MF(1, 1)) + (w2_ * MF(2, 1) + w3 * MF(3, 1));
    const f32x4 v = (__builtin_bit_cast(f32x4, a[0]) * w0 + __builtin_bit_cast(f32x4, a[1]) * w1_) + (__builtin_bit_cast(f32x4, a[2]) * w2_ + __builtin_bit_cast(f32x4, a[3]) * w3);
    float v0 = v[0], v1 = v[1], v2 = v[2], v3 = v[3];
#define ROR_ADD(n) "v_add_f32_dpp %0, %0, %0 row_ror:" n " row_mask:0xf bank_mask:0xf\n\tv_add_f32_dpp %1, %1, %1 row_ror:" n " row_mask:0xf bank_mask:0xf\n\t" \
      "v_add_f32_dpp %2, %2, %2 row_ror:" n " row_mask:0xf bank_mask:0xf\n\tv_add_f32_dpp %3, %3, %3 row_ror:" n " row_mask:0xf bank_mask:0xf\n\t" \
      "v_add_f32_dpp %4, %4, %4 row_ror:" n " row_mask:0xf bank_mask:0xf\n\t"
    asm("s_nop 1\n\t" ROR_ADD("8") ROR_ADD("4") ROR_ADD("2") : "+v"(L), "+v"(v0), "+v"(v1), "+v"(v2), "+v"(v3));
    const float rl = __builtin_amdgcn_rcpf(L);
    const unsigned p0 = pk_bf16(v0 * rl, v1 * rl), p1 = pk_bf16(v2 * rl, v3 * rl);
    const u32x4 of = {p0, p1, (unsigned)__builtin_amdgcn_mov_dpp((int)p0, XOR1, 0xf, 0xf, true), (unsigned)__builtin_amdgcn_mov_dpp((int)p1, XOR1, 0xf, 0xf, true)};
    const f32x4 z = {0.f, 0.f, 0.f, 0.f};
    const f32x4 y0 = mfma16(wof[0], of, z), y1 = mfma16(wof[1], of, z), y2 = mfma16(wof[2], of, z), y3 = mfma16(wof[3], of, z);
    if (c == 0) {
      LAS f32x4* yp = (LAS f32x4*)(L8 + a_ob);
      yp[0] = y0; yp[4] = y1; yp[8] = y2; yp[12] = y3;
    }
  }
  BAR();
  if (tw == 0) {
    const LAS f32x4* yp = (const LAS f32x4*)(L8 + a_yr);
    const f32x4 y = (yp[0] + yp[16]) + (yp[32] + yp[48]);
    const u32x2 uh = {min(pk_bf16(y[0], y[1]), SENT - 1u), min(pk_bf16(y[2], y[3]), SENT - 1u)};
    if (lane < 16) __builtin_amdgcn_raw_buffer_store_b64(uh, rs, o_y16, 0, AUX_SC1);
    __builtin_amdgcn_sched_barrier(0);
    u32x4 ub = __builtin_bit_cast(u32x4, y);
#pragma unroll
    for (int k = 0; k < 4; ++k) ub[k] = min(ub[k], SENT - 1u);
    if (lane < 16) __builtin_amdgcn_raw_buffer_store_b128(ub, rs, o_y, 0, AUX_SC1);
  }
  { const u64 tp = rt(); _Pragma("nounroll") while (rt() - tp < CD) {} }
  {
    f32x2 at;
    u32x4 yv[8];
#define C_GATHER() do { CFENCE(); \
      _Pragma("unroll") for (int k = 0; k < 8; ++k) yv[k] = __builtin_amdgcn_raw_buffer_load_b128(rs, o_c, k * 4096, AUX_SC1); \
      float s[8]; \
      _Pragma("unroll") for (int j = 0; j < 8; ++j) s[j] = 0.f; \
      _Pragma("unroll") for (int k = 0; k < 8; ++k) { \
        _Pragma("unroll") for (int j = 0; j < 8; ++j) s[j] = dot2(yv[k][j >> 1], (j & 1) ? sel_hi : sel_lo, s[j]); \
        __builtin_amdgcn_sched_barrier(0); } \
      _Pragma("unroll") for (int j = 0; j < 4; ++j) s[j] = swap32_add(s[j], s[j + 4]); \
      at = f32x2{swap16_add(s[0], s[2]), swap16_add(s[1], s[3])}; } while (0)
    C_GATHER();
    if (__builtin_expect(__any(__builtin_isunordered(at[0], at[1])), 0)) {
      _Pragma("nounroll") for (int sp = 0; sp < SPIN_CAP; ++sp) {
        unsigned mx = 0;
#pragma unroll
        for (int k = 0; k < 8; ++k) mx = max(mx, umax4(yv[k]));
        if (!__any(mx == SENT)) break;
        C_GATHER();
        if (!__any(__builtin_isunordered(at[0], at[1]))) break;
      }
    }
    const f32x2 hs = xf + rbf2(at);
    const unsigned hp = pk_bf16(hs[0], hs[1]);
    *(LAS float*)(L8 + a_st) = row_sum2(dot2(hp, 0x3f803f80u, 0.f), dot2(hp, hp, 0.f));
  }
  BAR();
#define Q4(base) ((*(const LAS f32x4*)(L8 + (base)) + *(const LAS f32x4*)(L8 + (base) + 16)) + (*(const LAS f32x4*)(L8 + (base) + 32) + *(const LAS f32x4*)(L8 + (base) + 48)))
  f32x2 hh = *(const LAS f32x2*)(L8 + a_fin + 64);
  {
    f32x4 sq = Q4(a_fin);
    asm volatile("" : "+v"(sq), "+v"(hh));
    const float mean = (sq[0] + sq[1]) * (1.0f / D);
    const float var = fmaxf(fmaf(-mean, mean, (sq[2] + sq[3]) * (1.0f / D)), 0.f);
    const float mb = rbf(mean), rden = ln_rden(var);
    const float mb2 = rbf((mean - mb) * rden), rden2 = ln_rden(var * rden * rden);
    const f32x2 tt = ln_apply2(ln_apply2(hh, mb, rden), mb2, rden2);
    f32x2 a0 = {0.f, 0.f}, a1 = {0.f, 0.f};
#pragma unroll
    for (int i = 0; i < 7; ++i) { const f32x2 gl = gelu2v(tt * w1f[i] + b1f[i]) * w2f[i]; if (i & 1) a1 += gl; else a0 += gl; }
    a0 += a1;
    *(LAS float*)(L8 + a_st + (L_RED - L_STAT)) = row_sum2(a0[0], a0[1]);
  }
  BAR();
  if (wave == HELPER) {
    const f32x4 r4 = Q4(a_fin + (L_RED - L_STAT));
    const f32x2 r = hh + rbf2(rbf2(f32x2{r4[0] + r4[1], r4[2] + r4[3]}) + bbf);
    if (lane == 0) *(GAS unsigned*)(out + 2 * pj) = pk_bf16(r[0], r[1]);
  }
}

extern "C" void kernel_launch(void* const* d_in, const int* in_sizes, int n_in, void* d_out, int out_size, void* d_ws, size_t ws_size, hipStream_t stream) {
  (void)hipFuncSetAttribute((const void*)k_fused, hipFuncAttributeMaxDynamicSharedMemorySize, L_END);
  hipLaunchKernelGGL(k_fused, dim3(NWG), dim3(512), L_END, stream, (const u16*)d_in[0], (const u16*)d_in[1], (const u16*)d_in[2], (const u16*)d_in[3],
                     (const u16*)d_in[4], (const u16*)d_in[6], (const u16*)d_in[7], (const u16*)d_in[8], (const u16*)d_in[9], (u16*)d_out, (unsigned char*)d_ws);
}
```
